```python
import torch

def _softmax(x, axis=-1):
    return torch.softmax(x, dim=axis)
def _gelu(x):
    return torch.nn.functional.gelu(x, approximate="tanh")
def _silu(x):
    return torch.nn.functional.silu(x)
def _sigmoid(x):
    return torch.sigmoid(x)
def _layernorm(x, axis=-1, eps=1e-5):
    m = x.mean(dim=axis, keepdim=True)
    v = x.var(dim=axis, keepdim=True, unbiased=False)
    return (x - m) / torch.sqrt(v + eps)
def _scatter_add(tgt, idx, src, axis=0):
    return tgt.clone().index_add_(axis, idx, src)
def _segment_sum(src, seg, n):
    out = torch.zeros((n,) + src.shape[1:], dtype=src.dtype)
    return out.index_add_(0, seg, src)
def _pad(x, pw):
    flat = tuple(v for pair in reversed(pw) for v in pair)
    return torch.nn.functional.pad(x, flat)

kernel_name = 'standalone__deep_cross_fibinet__recsys'

def setup_inputs():

    _w = (lambda a: (lambda t: t.to(torch.bfloat16) if t.is_floating_point() else t)(torch.as_tensor(a)))
    B, F, D = 4096, 26, 64
    V = min(100000, 100000)
    Fr = max(F // 2, 1)
    H = min(F * D, 256)
    return {
        "idx": _w((lambda lo, hi, *s: torch.randint(lo, hi, s))(0, V, B, F).to(torch.int64)),
        "table": _w((lambda *s: torch.randn(*s, dtype=torch.bfloat16) * 1.6)(V, D)),
        "se_w1": _w((lambda *s: torch.randn(*s, dtype=torch.bfloat16) * 1.6)(F, Fr)),
        "se_w2": _w((lambda *s: torch.randn(*s, dtype=torch.bfloat16) * 1.6)(Fr, F)),
        "w_bi": _w((lambda *s: torch.randn(*s, dtype=torch.bfloat16) * 0.05)(D, D) * (D ** -0.5)),
        "w_cross": _w((lambda *s: torch.randn(*s, dtype=torch.bfloat16) * 0.05)(2 * F * F * D) * 0.01),
        "w_deep1": _w((lambda *s: torch.randn(*s, dtype=torch.bfloat16) * 0.8)(F * D, H) * ((F * D) ** -0.5)),
        "w_deep2": _w((lambda *s: torch.randn(*s, dtype=torch.bfloat16) * 0.05)(H) * 0.01),
    }

def reference(idx, table, se_w1, se_w2, w_bi, w_cross, w_deep1, w_deep2):
    emb = table[idx]
    B = emb.shape[0]
    z = emb.mean(axis=-1)
    a = _sigmoid(_gelu(z @ se_w1) @ se_w2)
    v = a[:, :, None] * emb
    eW = torch.einsum('bfd,de->bfe', emb, w_bi)
    vW = torch.einsum('bfd,de->bfe', v, w_bi)
    p = torch.einsum('bie,bje->bije', eW, emb).reshape(B, -1)
    q = torch.einsum('bie,bje->bije', vW, v).reshape(B, -1)
    cross_feat = torch.concatenate([p, q], axis=-1)
    logit_cross = cross_feat @ w_cross
    h = _gelu(emb.reshape(B, -1) @ w_deep1)
    logit_deep = h @ w_deep2
    return logit_cross + logit_deep
```

```cpp
#define PFC 143
#include <hip/hip_runtime.h>
#include <hip/hip_bf16.h>
#include <stdint.h>

#define NF 26
#define NP 13
#define PROD_CLASS(slot) ((((slot) & 3) == 1) && (((slot) >> 2) & 1) == 0)
#define STAMP(k)
#define TS(x)
#define TP(x)
#define TE(x) TS(x)
using bf16x8 = __attribute__((ext_vector_type(8))) short;
using f32x4 = __attribute__((ext_vector_type(4))) float;
using f32x2 = __attribute__((ext_vector_type(2))) float;
using s16x4 = __attribute__((ext_vector_type(4))) short;
using u32x4 = __attribute__((ext_vector_type(4))) unsigned;
using u32x8 = __attribute__((ext_vector_type(8))) unsigned;
using u32x2 = __attribute__((ext_vector_type(2))) unsigned;
using bf2 = __attribute__((ext_vector_type(2))) __bf16;
#define PIN(x) asm volatile("" : "+v"(x))
#define PINS(x) asm volatile("" : "+s"(x))
#define SB __builtin_amdgcn_sched_barrier(0);
#define CAT8(a, b) __builtin_shufflevector((a), (b), 0, 1, 2, 3, 4, 5, 6, 7)

#define SLOT 16384
#define OFF_B 65536
#define OFF_FL 131072
#define OFF_WBI (OFF_FL + 4096)
#define OFF_SW1 (OFF_WBI + 2048)
#define OFF_SW2 (OFF_SW1 + 1024)
#define OFF_WD2 (OFF_SW2 + 1024)
#define OFF_TAG (OFF_WD2 + 256)
#define OFF_NB (OFF_TAG + 768)
#define OFF_JNK (OFF_NB + 2048)
#define OFF_IDX (OFF_JNK + 2048)
#define OFF_RAW (OFF_IDX + 8192)
#define LDS_T (OFF_IDX + 16384)
constexpr int FOFF[4] = {OFF_B + SLOT, SLOT, OFF_B + 2 * SLOT, 2 * SLOT};
#define P_FLAG 3
#define P_POLL 6
#define P_CHK (P_POLL + 3)
#define P_STAT 9

#define WS_FLAG 1024
#define WS_SUM 4096
#define WS_SIDE 65536
#define WS_FRAG (1 << 20)

#define LDS(T, a) (*(__attribute__((address_space(3))) T*)(size_t)(a))
#define LDSV(a) (*(volatile const __attribute__((address_space(3))) unsigned*)(size_t)(a))
__device__ __forceinline__ unsigned pk_bf16(float lo, float hi) {
    const f32x2 v = {lo, hi};
    return __builtin_bit_cast(unsigned, __builtin_convertvector(v, bf2));
}
__device__ __forceinline__ unsigned cvt_o(float lo, float hi) { unsigned r; asm volatile("v_cvt_pk_bf16_f32 %0, %1, %2" : "=v"(r) : "v"(lo), "v"(hi)); return r; }
__device__ __forceinline__ float exp_o(float a) { float r; asm volatile("v_exp_f32 %0, %1" : "=v"(r) : "v"(a)); return r; }
template <typename A, typename B> __device__ __forceinline__ f32x4 mf0(const A& a, const B& b) { f32x4 d; asm volatile("v_mfma_f32_16x16x32_bf16 %0, %1, %2, 0" : "=v"(d) : "v"(a), "v"(b)); return d; }
template <typename A, typename B> __device__ __forceinline__ void mfa(f32x4& d, const A& a, const B& b) { asm volatile("v_mfma_f32_16x16x32_bf16 %0, %1, %2, %0" : "+v"(d) : "v"(a), "v"(b)); }
__device__ __forceinline__ float lo_f(unsigned x) { return __uint_as_float(x << 16); }
__device__ __forceinline__ float hi_f(unsigned x) { return __uint_as_float(x & 0xffff0000u); }
__device__ __forceinline__ s16x4 tr_read(unsigned a) {
    typedef short v4s __attribute__((ext_vector_type(4)));
    return __builtin_amdgcn_ds_read_tr16_b64_v4i16((__attribute__((address_space(3))) v4s*)(size_t)a);
}
#define DMA(name, ins) __device__ __forceinline__ void name(unsigned voff, const void* sbase, unsigned ldsaddr) { \
    asm volatile("s_mov_b32 m0, %2\n\ts_nop 0\n\t" ins :: "v"(voff), "s"(sbase), "s"(ldsaddr) : "memory"); }
DMA(dma16, "global_load_lds_dwordx4 %0, %1") DMA(dma4, "global_load_lds_dword %0, %1")
DMA(dma16sc, "global_load_lds_dwordx4 %0, %1 sc1") DMA(dma4sc, "global_load_lds_dword %0, %1 sc1")
__device__ __forceinline__ void st16sc(unsigned voff, void* sbase, u32x4 v) {
    asm volatile("global_store_dwordx4 %0, %1, %2 sc1\n\ts_nop 1" :: "v"(voff), "v"(v), "s"(sbase) : "memory");
}
template <int O> __device__ __forceinline__ void set_m0(unsigned base) { asm volatile("s_add_u32 m0, %0, %1" :: "s"(base), "n"(O) : "memory", "scc"); }
template <int O> __device__ __forceinline__ void bdma16o(unsigned voff, __amdgpu_buffer_rsrc_t rs, unsigned soff) {
    asm volatile("buffer_load_dwordx4 %0, %1, %2 offen offset:%3 lds" :: "v"(voff), "s"(rs), "s"(soff), "n"(O) : "memory");
}
template <int O> __device__ __forceinline__ void dma16o(unsigned voff, const void* sbase) {
    asm volatile("global_load_lds_dwordx4 %0, %1 offset:%2" :: "v"(voff), "s"(sbase), "n"(O) : "memory");
}
template <int O> __device__ __forceinline__ void dma16sco(unsigned voff, const void* sbase) {
    asm volatile("global_load_lds_dwordx4 %0, %1 offset:%2 sc1" :: "v"(voff), "s"(sbase), "n"(O) : "memory");
}
#define WAITCNT(vm, lgkm) __builtin_amdgcn_s_waitcnt(((vm) & 15) | (((vm) >> 4) << 14) | 0x70 | ((lgkm) << 8))
#define WAITX(vm, x, m) { WAITCNT((vm) + (x), 0); asm volatile("s_cmp_eq_u32 %0, 0\n\ts_cbranch_scc1 1f\n\ts_waitcnt vmcnt(%1)\n1:" :: "s"(m), "n"(vm) : "memory", "scc"); }
#define BARRIER asm volatile("s_barrier" ::: "memory")
__device__ __forceinline__ float sum32(float v) { const u32x2 p = __builtin_amdgcn_permlane32_swap(__float_as_uint(v), __float_as_uint(v), false, false); return __uint_as_float(p[0]) + __uint_as_float(p[1]); }
__device__ __forceinline__ float sum16(float v) { const u32x2 p = __builtin_amdgcn_permlane16_swap(__float_as_uint(v), __float_as_uint(v), false, false); return __uint_as_float(p[0]) + __uint_as_float(p[1]); }
__device__ __forceinline__ float sel(unsigned m, float a, float b) {
    return __uint_as_float(__builtin_amdgcn_bitop3_b32(m, __float_as_uint(a), __float_as_uint(b), 0xca));
}
__device__ __forceinline__ void wait_set(unsigned f, unsigned a) {
    if (__builtin_amdgcn_ballot_w64(f == 0u) != 0ull) {
#pragma unroll 1
        for (int it = 0; it < (1 << 20) && __builtin_amdgcn_ballot_w64(LDSV(a) == 0u) != 0ull; ++it) { }
    }
    asm volatile("" ::: "memory");
}

struct Com {
    unsigned long long pa, pb;
    char* ws;
    unsigned lane16, ldsb;
    int c, nq, rho, st;
};
typedef __attribute__((address_space(1))) char* gp;
__device__ __forceinline__ void deliver(const Com& c, float v, int part, unsigned s2, unsigned s8, double big, gp sums, gp outp) {
    const gp side = (gp)c.ws + (WS_SIDE + c.st * 2048 + c.c * 64);
    u32x2 mine = __builtin_bit_cast(u32x2, __builtin_fma((double)v, 0x1p42, big));
    mine[1] += 0x04004000u - 0x43380000u;
    const unsigned ab = __float_as_uint(v) << 1;
    if (__builtin_expect(ab - 0x61000000u >= 0x22000000u && ab != 0u, 0)) {
        mine = u32x2{0u, 0x04004000u + (0x40000u << part)};
        __hip_atomic_store((__attribute__((address_space(1))) float*)(side + part * 256 + s2 * 2), v, __ATOMIC_RELAXED, __HIP_MEMORY_SCOPE_AGENT);
        asm volatile("s_waitcnt vmcnt(0)" ::: "memory");
    }
    const unsigned long long mv = __builtin_bit_cast(unsigned long long, mine);
    const unsigned long long tot = mv + __hip_atomic_fetch_add((__attribute__((address_space(1))) unsigned long long*)(sums + s8), mv, __ATOMIC_RELAXED, __HIP_MEMORY_SCOPE_AGENT);
    const unsigned th = (unsigned)(tot >> 32);
    if (th >= 0x20000000u) {
        const u32x2 dv = {(unsigned)tot, (th & 0x3ffffu) | 0x43300000u};
        float r = (float)__builtin_fma(__builtin_bit_cast(double, dv), 0x1p-42, -0x1.2p10);
        if (__builtin_expect(th >= 0x20040000u, 0)) {
#pragma unroll 1
            for (int k = 0; k < 8; ++k)
                if ((th >> (18 + k)) & 1u) r += __hip_atomic_load((__attribute__((address_space(1))) float*)(side + k * 256 + s2 * 2), __ATOMIC_RELAXED, __HIP_MEMORY_SCOPE_AGENT);
        }
        *(__attribute__((address_space(1))) uint16_t*)(outp + s2) = (uint16_t)pk_bf16(r, r);
    }
}

struct Deep : Com {
    __amdgpu_buffer_rsrc_t rw1;
    const char* wsf; const char* wd2;
    unsigned sG[4], sB;
    unsigned m1, m3, m4;
    int unit;
    unsigned long long pmask;
    unsigned tad, tag, pl;
    unsigned boff[4], ra[4], rb[4];
    float w2[16];
    f32x4 ac[4][4];
    bf16x8 fa[2][4];
    s16x4 lo[2][4], hi[2][4];
};
template <int Q> __device__ __forceinline__ void prepB(Deep& c) {
    if constexpr (Q == 0) c.sB = 24 * 32768; else c.sB = c.sG[(Q - 1) & 3] + ((Q - 1) >> 2) * (8 * 32768);
    PINS(c.sB);
    set_m0<OFF_B + (Q & 3) * SLOT>(c.ldsb);
}
template <int I> __device__ __forceinline__ void issueB(Deep& c) { bdma16o<I * 1024>(c.boff[I], c.rw1, c.sB); }
__device__ __forceinline__ void mkboff(Deep& d, int lane, int cw, int nq) {
    const unsigned b0 = (unsigned)(((cw & 1) * 64 + (cw >> 1) * 32 + (lane >> 3)) * 512 + nq * 128 + ((lane & 7) ^ ((lane >> 3) & 2)) * 16);
    d.boff[0] = b0; d.boff[1] = (b0 ^ 64u) + 3072; d.boff[2] = b0 + 6144; d.boff[3] = (b0 ^ 64u) + 9216;
#pragma unroll
    for (int k = 0; k < 4; ++k) PIN(d.boff[k]);
}
template <int P, int N> __device__ __forceinline__ void rd1(Deep& c) {
    constexpr int tab[12] = {0, 4, 5, 6, 7, 1, 2, 3, 8, 9, 10, 11}, k = tab[N < 12 ? N : 0];
    if constexpr (N >= 12) { }
    else if constexpr (k < 4) c.fa[P & 1][k] = LDS(const bf16x8, c.ra[k] + (P & 3) * SLOT);
    else if constexpr (k & 1) c.hi[P & 1][(k - 4) >> 1] = tr_read(c.rb[(k - 4) >> 1] + (P & 3) * SLOT + 512);
    else c.lo[P & 1][(k - 4) >> 1] = tr_read(c.rb[(k - 4) >> 1] + (P & 3) * SLOT);
    SB
}
template <int P, int M, int K> __device__ __forceinline__ void mk(Deep& c) {
    if constexpr (P >= 0) mfa(c.ac[M][K], CAT8(c.lo[P & 1][K], c.hi[P & 1][K]), c.fa[P & 1][M]);
    SB
}
template <int P, int N> __device__ __forceinline__ void m1(Deep& c) { mk<P, (N >> 1) & 3, (N >> 3) * 2 + (N & 1)>(c); }
template <int P, int M> __device__ __forceinline__ void m4(Deep& c) { mk<P, M, 0>(c); mk<P, M, 1>(c); mk<P, M, 2>(c); mk<P, M, 3>(c); }
#define LSTEP 0xbbaa
constexpr int lstep(int k) { return (LSTEP >> (4 * k)) & 15; }
constexpr int lfirst(int S) { int k = 0; while (k < 4 && lstep(k) < S) ++k; return k; }
constexpr int dops(int S) { return S < 8 ? 4 : 4 * (lfirst(S + 1) - lfirst(S)); }
static_assert(lstep(2) > P_CHK && lstep(3) > 10, "fragments: after the check, chunk 9 stays during step 10");
constexpr int dyoung(int S, int U) { return 4 * ((S < 8 ? S + 4 : 11 + lfirst(S + 1)) - (U + 1)) + (S == 1 || S == 2); }
template <int S, int I> __device__ __forceinline__ void dissue1(Deep& c) {
    constexpr int U = S < 8 ? S + 3 : 11 + lfirst(S) + (I >> 2);
    if constexpr (U < NP) {
        if constexpr ((I & 3) == 0) { prepB<U>(c); asm volatile("s_nop 0"); }
        issueB<I & 3>(c);
    } else {
        if constexpr ((I & 3) == 0) { set_m0<FOFF[U - NP]>(c.ldsb); asm volatile("s_nop 0"); }
        dma16sco<(I & 3) * 1024>(c.lane16, c.wsf + (U - NP) * 16384);
    }
    SB
}
template <int S, int I0, int I1> __device__ __forceinline__ void dissue(Deep& c) {
    if constexpr (I0 < I1 && I0 < dops(S)) { dissue1<S, I0>(c); dissue<S, I0 + 1, I1>(c); }
}
__device__ __forceinline__ unsigned tag_addr(int lane, int unit, int nq, int st, int xcd) {
    const unsigned l4 = (unsigned)lane * 4u;
    const unsigned a0 = (unsigned)(WS_FLAG + unit * 4) + l4 * 32u;
    const unsigned a1 = (((unsigned)(nq * 256) + l4 * 64u) & 768u) + (unsigned)(st * 4);
    const unsigned a2 = (unsigned)(WS_FLAG + xcd * 128 - 64) + l4;
    const unsigned lo = l4 < 32u ? a0 : a1, hi = l4 < 168u ? a2 : (unsigned)(st * 4);
    return l4 < 64u ? lo : hi;
}
__device__ __forceinline__ void publish(Deep& c, unsigned sv) {
    asm volatile("s_cmp_eq_u64 %3, 0\n\ts_cbranch_scc1 1f\n\ts_mov_b64 exec, %3\n\tglobal_store_dword %0, %1, %2 sc1\n\ts_mov_b64 exec, -1\n1:"
                 :: "v"(c.tad), "v"(sv), "s"(c.ws), "s"(c.pmask) : "memory", "scc");
}
__device__ __forceinline__ void produce(Deep& c) {
    if (c.m1 == 0u) {
        const int unit = c.unit;
        const unsigned p8 = (c.lane16 << 4) & 0x300u, e2 = (c.lane16 >> 5) & 0x1eu;
        const unsigned rb = (unsigned)(OFF_RAW + c.c * 32) + e2;
        const unsigned dm = p8 == (c.lane16 & 0x300u) ? ~0u : 0u;
        const int m = unit < 24 ? 2 * (unit & 6) + (unit & 1) + ((unit >> 2) & 2) : 4 * (unit & 1) + 2;
        unsigned fo = (unsigned)(WS_FRAG + c.c * 65536 + (unit >> 4) * 1024 + m * 64) + e2 * 2048u + ((c.lane16 & 0x30u) ^ (unsigned)((m & 4) << 3));
        if (unit >= 24) fo += c.lane16 & 0x200u;
#pragma unroll
        for (int pq = 0; pq < 2; ++pq) {
            u32x4 fr;
#pragma unroll
            for (int d = 0; d < 4; ++d) {
                const unsigned rp = rb + (d < 3 ? p8 : 0u) + (unsigned)(pq * (NF * 128) + 1024 * d);
                fr[d] = LDS(const uint16_t, rp) | ((unsigned)LDS(const uint16_t, rp + 128) << 16);
            }
            fr[3] &= dm;
            st16sc(fo + pq * 2048u, c.ws, fr);
        }
        if (unit >= 24) { st16sc(fo ^ 512u, c.ws, u32x4{0u, 0u, 0u, 0u}); st16sc((fo ^ 512u) + 2048u, c.ws, u32x4{0u, 0u, 0u, 0u}); }
    }
    SB
}
__device__ __forceinline__ void check_flags(Deep& c) {
    if (c.m3 == 0u) {
        const unsigned l4 = c.lane16 >> 2;
        const unsigned long long open = __builtin_amdgcn_ballot_w64(c.tag == c.pl) & (c.nq ? 0x7ffffff0000ull : 0x3ffffff0000ull);
        if (__builtin_expect(open != 0ull, 0)) {
#pragma unroll 1
            for (int it = 0; it < (1 << 22); ++it) {
                dma4sc(c.tad, c.ws, OFF_TAG + 256);
                asm volatile("s_waitcnt vmcnt(0)" ::: "memory");
                if ((__builtin_amdgcn_ballot_w64(LDSV(OFF_TAG + 256 + l4) == c.tag) & open) == 0ull) break;
                __builtin_amdgcn_s_sleep(2);
            }
        }
    }
    SB
}
__device__ __forceinline__ void fetch_status(Deep& c) {
    if (c.m4 == 0u) {
        if (c.nq) dma16sc(c.lane16, c.ws, OFF_NB + 1024);
        else dma4sc(c.tad, c.ws, OFF_TAG + 512);
    }
    SB
}
constexpr int x3(int P) { return (P == P_FLAG || P == P_FLAG + 1) + (P == P_POLL || P == P_POLL + 1); }
#define DTOP 2
#define DTOPL 4
#define DTOPB 4
constexpr int dtop(int P) { return P < 8 ? ((P & 3) == 2 ? DTOPB : DTOP) : DTOPL; }
template <int P, int N> __device__ __forceinline__ void dbody(Deep& c) {
    constexpr int top = dtop(P);
    m1<P - 1, N>(c);
    if constexpr (P > 10) {
        if constexpr (N < 4) rd1<P, (N ? N + 4 : 0)>(c);
        else if constexpr (N < 7) dissue<P, top + N - 4, top + N - 3>(c);
        else if constexpr (N < 15) {
            if constexpr (N == 7) { WAITCNT(dyoung(P - 1, P) + (dops(P) < top + 3 ? dops(P) : top + 3), 15); SB }
            rd1<P, (N < 11 ? N - 6 : N - 3)>(c);
        } else dissue<P, top + 3, 16>(c);
    } else {
        rd1<P, N>(c);
        if constexpr (N >= 12) dissue<P, top + N - 12, (N == 15 ? 16 : top + N - 11)>(c);
    }
    if constexpr (N < 15) dbody<P, N + 1>(c);
}
template <int P> struct DStep {
    static __device__ __forceinline__ void run(Deep& c) {
        TS(c.ts[P])
        SB
        if constexpr (P == P_FLAG) publish(c, c.tag + 1u);
        if constexpr (P == 1) { dma4((unsigned)(c.nq * 128) + ((c.lane16 >> 2) & 124u), c.wd2, OFF_WD2); SB }
        if constexpr (P == P_CHK) check_flags(c);
        if constexpr (P == P_STAT) fetch_status(c);
        dissue<P, 0, dtop(P)>(c);
        dbody<P, 0>(c);
        if constexpr (P <= 10) {
            if constexpr (P == 4) {
#pragma unroll
                for (int k = 0; k < 4; ++k) {
                    const u32x2 v = LDS(const u32x2, (unsigned)(OFF_WD2 + k * 32) + ((c.lane16 >> 5) & 24u));
                    c.w2[4 * k] = lo_f(v[0]); c.w2[4 * k + 1] = hi_f(v[0]); c.w2[4 * k + 2] = lo_f(v[1]); c.w2[4 * k + 3] = hi_f(v[1]);
                }
#pragma unroll
                for (int k = 0; k < 16; ++k) PIN(c.w2[k]);
            }
            if constexpr (P == P_POLL) { if (c.m3 == 0u) dma4sc(c.tad, c.ws, OFF_TAG + 256); }
            SB
            if constexpr (P + 1 < P_FLAG && P < 2) WAITX(dyoung(P, P + 1), 2, c.m1)
            else if constexpr (P == P_STAT) WAITX(dyoung(P, P + 1), 1, c.m4)
            else if constexpr (x3(P) > 0) WAITX(dyoung(P, P + 1), x3(P), c.m3)
            else if constexpr (P == 10) WAITCNT(63, 0);
            else WAITCNT(dyoung(P, P + 1), 0);
            if constexpr (P == P_CHK - 1) { SB c.pl = LDSV(OFF_TAG + 256 + (c.lane16 >> 2)); SB }
            BARRIER;
        }
        if constexpr (P + 1 < NP) DStep<P + 1>::run(c);
    }
};

struct Cross : Com {
    const char* tb[4]; const char* wsf;
    unsigned ga[40];
    unsigned re[2], rx[2], rx0, six, wf[2];
    s16x4 wa[2];
    bf16x8 wb[2], wr[2];
    u32x4 fe[3][2][2];
    f32x4 ew[NF], zs[4];
    u32x4 X[NP], bp[4];
    float k0, k1, k2;
    unsigned am;
    u32x4 on[4], s1, zq;
};
template <int I> __device__ __forceinline__ void issueA(Cross& c, unsigned off) { dma16o<I * 1024>(off, c.tb[I]); }
template <int K, int S, int N> __device__ __forceinline__ void sw1(Cross& c) {
    constexpr int a = 1 + 4 * K + (S == 1 ? 2 : (S == 3 ? 1 : 0)), b = 1 + 4 * K + (S == 0 ? 1 : (S == 2 ? 2 : 3)), Q = N >> 2, R = N & 3;
    float x = c.ew[2 * a + Q][R], y = c.ew[2 * b + Q][R];
    if (S >= 2) asm volatile("v_permlane32_swap_b32 %0, %1" : "+v"(x), "+v"(y)); else asm volatile("v_permlane16_swap_b32 %0, %1" : "+v"(x), "+v"(y));
    c.ew[2 * a + Q][R] = x; c.ew[2 * b + Q][R] = y;
}
template <int K, int S, int N0 = 0, int N1 = 8> __device__ __forceinline__ void swn(Cross& c) { if constexpr (N0 < N1) { sw1<K, S, N0>(c); swn<K, S, N0 + 1, N1>(c); } }
constexpr int sw0(int P) { return P <= 3 ? 0 : P <= 5 ? 8 : P == 6 ? 20 : P == 7 ? 32 : P <= 9 ? 40 : P == 10 ? 52 : 64; }
template <int P, int N0, int N1> __device__ __forceinline__ void tsw(Cross& c) {
    if constexpr (N0 < N1 && sw0(P) + N0 < sw0(P + 1)) { sw1<((sw0(P) + N0) >> 5), ((sw0(P) + N0) >> 3) & 3, (sw0(P) + N0) & 7>(c); tsw<P, N0 + 1, N1>(c); }
}
constexpr int fbuf(int P) { return P == 12 ? 2 : P & 1; }
template <int P, int N> __device__ __forceinline__ void cmm(Cross& c) {
    if constexpr (P >= 0) {
        constexpr int B = fbuf(P);
        if constexpr (N < 2)
            asm volatile("v_smfmac_f32_16x16x64_bf16 %0, %1, %2, %3" : "+v"(c.zs[P ? (P - 1) >> 2 : 3])
                         : "v"(c.on[P ? (P - 1) & 3 : 0]), "v"(__builtin_shufflevector(c.fe[B][N][0], c.fe[B][N][1], 0, 1, 2, 3, 4, 5, 6, 7)), "v"(c.six));
        else if constexpr (N < 4) c.ew[2 * P + (N & 1)] = mf0(P ? c.wb[0] : c.wr[0], c.fe[B][0][N & 1]);
        else mfa(c.ew[2 * P + (N & 1)], P ? c.wb[1] : c.wr[1], c.fe[B][1][N & 1]);
    }
    SB
}
template <int P, int N> __device__ __forceinline__ void rdE(Cross& c) {
    c.fe[fbuf(P)][N >> 1][N & 1] = LDS(const u32x4, c.re[N >> 1] + (P & 3) * SLOT + (N & 1) * 2048);
    SB
}
template <int K> __device__ __forceinline__ void zpack(Cross& c) {
    float a = c.zs[K][0], b = c.zs[K][2];
    unsigned r;
    asm volatile("v_add_f32 %1, %1, %3\n\tv_add_f32 %2, %2, %4\n\tv_cvt_pk_bf16_f32 %0, %1, %2" : "=v"(r), "+v"(a), "+v"(b) : "v"(c.zs[K][1]), "v"(c.zs[K][3]));
    c.zq[K] = r;
    SB
}
template <int K> __device__ __forceinline__ void zclear(Cross& c) { c.zs[K] = f32x4{0.f, 0.f, 0.f, 0.f}; PIN(c.zs[K]); SB }
template <int P> __device__ __forceinline__ void rdX(Cross& c) {
    constexpr int k = 1 + ((P - 1) & ~3);
    if constexpr (P == 0) c.X[0] = LDS(const u32x4, c.rx0);
    else if constexpr ((P & 3) == 2)
        asm volatile("s_mov_b32 exec_hi, 0\n\tds_read_b128 %0, %4\n\tds_read_b128 %1, %4 offset:2048\n\tds_read_b128 %2, %5\n\tds_read_b128 %3, %5 offset:2048\n\ts_mov_b32 exec_hi, -1"
                     : "=&v"(c.X[k]), "=&v"(c.X[k + 1]), "=&v"(c.X[k + 2]), "=&v"(c.X[k + 3]) : "v"(c.rx[0]), "v"(c.rx[1]) : "memory");
    else if constexpr ((P & 3) == 0)
        asm volatile("s_mov_b32 exec_lo, 0\n\tds_read_b128 %0, %4\n\tds_read_b128 %1, %4 offset:2048\n\tds_read_b128 %2, %5\n\tds_read_b128 %3, %5 offset:2048\n\ts_mov_b32 exec_lo, -1"
                     : "+v"(c.X[k]), "+v"(c.X[k + 1]), "+v"(c.X[k + 2]), "+v"(c.X[k + 3]) : "v"(c.rx[0]), "v"(c.rx[1]) : "memory");
    SB
}
template <int P> struct CStep {
    static __device__ __forceinline__ void run(Cross& c) {
        TS(c.ts[P])
        SB
        cmm<P - 1, 2>(c); rdE<P, 0>(c);
        cmm<P - 1, 3>(c); rdE<P, 1>(c);
        cmm<P - 1, 4>(c); rdE<P, 2>(c);
        cmm<P - 1, 5>(c); rdE<P, 3>(c);
        cmm<P - 1, 0>(c);
        if constexpr (P + 3 < NP) set_m0<((P + 3) & 3) * SLOT>(c.ldsb);
        cmm<P - 1, 1>(c);
        rdX<P>(c);
        constexpr bool xw = P && !(P & 1);
#define GA(J) if constexpr (P + 3 < NP) issueA<J>(c, c.ga[4 * P + (J)]); SB
        if constexpr (P == 4 || P == 8) zclear<P / 4>(c);
        if constexpr (P == 8) {
#pragma unroll
            for (int r = 0; r < 4; ++r) { c.bp[r][3] = cvt_o(c.ew[0][r], c.ew[1][r]); c.bp[r][0] = cvt_o(c.ew[2][r], c.ew[3][r]); }
            c.k0 = -2.0f * 0.7978845608028654f * 1.4426950408889634f / 64.0f; c.k1 = c.k0 * (0.044715f / 4096.0f); c.k2 = -64.0f / 1.4426950408889634f;
            c.am = (c.wf[0] & 16u) ? ~0u : 0u;
            PIN(c.k0); PIN(c.k1); PIN(c.k2); PIN(c.am);
            SB
        }
        if constexpr (P == 2 || P == 6 || P == 10) zpack<(P == 2 ? 3 : (P - 6) / 4)>(c);
        if constexpr (xw) { tsw<P, 0, 4>(c); WAITCNT(63, 0); GA(0) tsw<P, 4, 6>(c); GA(1) tsw<P, 6, 8>(c); GA(2) tsw<P, 8, 10>(c); GA(3) tsw<P, 10, 12>(c); }
        else { GA(0) GA(1) tsw<P, 0, 4>(c); GA(2) tsw<P, 4, 8>(c); GA(3) tsw<P, 8, 12>(c); }
        if constexpr (P == 3) {
            const unsigned r6 = c.lane16 * 4u + (unsigned)c.rho * 256u;
            const unsigned am = (c.lane16 >> 8) + (unsigned)c.rho;
            c.wf[0] = (r6 & 0x3c0u) | (((am << 4) ^ ((r6 >> 3) & 0x20u)) & 0x30u) | SLOT;
            c.wf[1] = c.wf[0] + OFF_B;
            const unsigned wap = OFF_SW2 + (c.lane16 & 0x3c0u) + ((c.lane16 >> 1) & 0x18u);
            c.wa[0] = tr_read(wap); c.wa[1] = tr_read(wap + 32);
            const unsigned r2 = (c.lane16 >> 3) & 30u, s1 = OFF_SW1 + min(r2, 24u), sg = s1 + (am & 3u) * 52u;
#pragma unroll
            for (int k = 0; k < 4; ++k) {
                const unsigned a = (k < 3 ? sg : s1) + k * 208;
                const unsigned v = LDS(const uint16_t, a) | ((unsigned)LDS(const uint16_t, a + 26) << 16);
                c.s1[k] = r2 < 26u ? v : 0u;
            }
            PIN(c.s1);
            PIN(c.wf[0]); PIN(c.wf[1]); PIN(c.wa[0]); PIN(c.wa[1]);
        }
        SB
        WAITCNT(P < 10 ? 8 : 0, 0); BARRIER;
        if constexpr (P < 10) CStep<P + 1>::run(c);
    }
};

#define EB 2
#define SD 0
#define SW 4
#define NB1 1
#define TN 7
#define TC 1
#define SF 1
#define WPRE 6
__global__ __launch_bounds__(512) void k_main(const int* __restrict__ idx, const uint16_t* __restrict__ table,
        const uint16_t* __restrict__ w_bi, const uint16_t* __restrict__ w_deep1, const uint16_t* __restrict__ w_cross,
        unsigned* __restrict__ ws32, const uint16_t* __restrict__ se_w1, const uint16_t* __restrict__ se_w2,
        const uint16_t* __restrict__ w_deep2_, __hip_bfloat16* __restrict__ out_, unsigned long long* __restrict__ stamps) {
    SB
    const int tid = threadIdx.x, lane = tid & 63;
    const int w = __builtin_amdgcn_readfirstlane(tid >> 6);
    const int bid = blockIdx.x;
    const int xcd = bid & 7, slot = bid >> 3;
    const int st = xcd * 8 + (slot >> 2), nq = slot & 3;
    const int s0 = st * 64;
    const int rho = (st + nq) & 3, cw = w & 3;
    const int l15 = lane & 15, g = lane >> 4;
    SB const unsigned long long zpa = __builtin_amdgcn_s_memrealtime(); SB
    LDS(u32x2, OFF_FL + tid * 8) = u32x2{0u, 0u};
    unsigned long long pc;
    {
        const unsigned ln = min((unsigned)(4 + w * 4 + (lane & 3) + ((lane >> 2) & 1) * (32 + nq * 32)), (unsigned)PFC) * 128u;
        asm volatile("s_getpc_b64 %0\n\ts_mov_b32 m0, %2\n\ts_mov_b64 exec, 0xff\n\tglobal_load_lds_dword %1, %0\n\ts_mov_b64 exec, -1"
                     : "=&s"(pc) : "v"(ln), "n"(OFF_JNK) : "memory");
    }
    SB
    float gc0 = -2.0f * 0.7978845608028654f * 1.4426950408889634f, gc1 = gc0 * 0.044715f;
#define FIN_ST(c)
#define FIN_SA(c)
#define FINISH(c) if ((1) && (1)) { const unsigned long long tb_ = __builtin_amdgcn_s_memrealtime(), tn = __builtin_amdgcn_s_memrealtime(), te = tn + 8 * (tb_ - zpa); while (__builtin_amdgcn_s_memrealtime() < te) __builtin_amdgcn_s_sleep(2); } STAMP(5) FIN_ST(c) FIN_SA(c) __builtin_amdgcn_endpgm(); __builtin_unreachable();
    unsigned lane16 = (unsigned)lane * 16u;
    int deep = w >> 2;
    Deep d;
    d.ldsb = (unsigned)(cw * 4096);
    unsigned o2 = min(lane16, 624u), im0 = (unsigned)(OFF_IDX + cw * 2048), io = (unsigned)((s0 + 16 * cw) * (NF * 4));
    mkboff(d, lane, cw, nq);
#pragma unroll
    for (int k = 0; k < 4; ++k) { d.sG[k] = (unsigned)(((k + rho) & 3) * (2 * 32768)); PINS(d.sG[k]); }
    d.unit = xcd + 8 * ((st & 7) >> 1);
    d.m1 = (unsigned)((nq ^ 1) | (st & 1) | ((d.unit + 32 - NF) >> 5));
    unsigned to = min(min(lane16, (TN - 1) * 16u) * 8u + (unsigned)((bid * 4 + cw) * (TN * 128)), NF * 32768u - 128u);
    unsigned tc = min(min(lane16, 16u) * 8u + (unsigned)((bid * 4 + cw) * 256), 2u * NF * NF * 128u - 128u);
    im0 = deep ? (unsigned)OFF_JNK : im0;
    PIN(lane16); PIN(o2); PIN(to); PINS(im0); PINS(io); PINS(deep); PINS(d.ldsb); PINS(d.m1);
    if (TC) PIN(tc);
    asm volatile("s_mov_b32 m0, %0" :: "s"(im0) : "memory");
    SB
    asm volatile("" :: "s"(idx), "s"(table), "s"(se_w1), "s"(se_w2), "s"(w_bi), "s"(w_deep1), "s"(w_cross), "s"(ws32));
    asm volatile("s_cmp_lg_u32 %3, 0\n\ts_cbranch_scc1 1f\n\tglobal_load_lds_dwordx4 %0, %2\n\tglobal_load_lds_dwordx4 %1, %2 offset:1024\n\ts_branch 2f\n1:\n\tglobal_load_lds_dword %4, %5\n2:"
                 :: "v"(lane16), "v"(o2), "s"((const char*)idx + io), "s"(deep), "v"(to), "s"(w_deep1) : "memory", "scc");
    if (TC) asm volatile("s_cmp_eq_u32 %2, 0\n\ts_cbranch_scc1 1f\n\tglobal_load_lds_dword %0, %1\n1:" :: "v"(tc), "s"(w_cross), "s"(deep) : "memory", "scc");
    SB
    typedef const __attribute__((address_space(4))) unsigned long long* kap;
    kap ka = (kap)__builtin_amdgcn_kernarg_segment_ptr();
    PINS(ka);
    d.wd2 = (const char*)ka[8];
    const gp out = (gp)ka[9];
    unsigned long long w1p;
    asm volatile("s_mov_b64 %0, %1" : "=s"(w1p) : "s"(w_deep1));
    d.rw1 = __builtin_amdgcn_make_buffer_rsrc((void*)w1p, (short)0, NF * 32768, 0x00020000);
    prepB<0>(d);
    TS(tz1)
    if (!NB1) BARRIER;
    TS(tz2)
    Cross x;
    x.c = cw; x.nq = nq; x.rho = rho; x.st = st; x.lane16 = lane16; x.ws = (char*)ws32; x.ldsb = d.ldsb;
    unsigned srcA, srcB, ib[4];
    u32x2 ia[NP], ic[NP];
    if (deep) {
        if (!SF) { issueB<0>(d); issueB<1>(d); issueB<2>(d); issueB<3>(d); }
        SB
        dma16((unsigned)((cw & 1) * 4096 + (lane >> 1) * 128 + nq * 32 + (lane & 1) * 16), w_bi, OFF_WBI + (cw & 1) * 1024);
        dma4((unsigned)min(cw * 64 + lane, 168) * 4u, se_w1, OFF_SW1 + cw * 256);
        const int k = (l15 & 1) | ((l15 >> 2) & 2), r = 4 * cw + g;
        dma4((unsigned)((r < 13 ? r : 12) * (NF * 2) + k * 16 + (k == 3 ? 0 : ((l15 + 2 * rho) & 6) * 2)), se_w2, OFF_SW2 + cw * 256);
        SB
        if (__builtin_expect(d.m1 == 0u, 1)) {
            const unsigned r0 = (unsigned)(16 * cw + (lane >> 3)), lo = (lane16 & 0x70u) + (unsigned)(d.unit * 128);
            dma16(min(r0, 2u * NF - 1) * (NF * 128) + lo, w_cross, OFF_RAW + cw * 2048);
            dma16(min(r0 + 8u, 2u * NF - 1) * (NF * 128) + lo, w_cross, OFF_RAW + cw * 2048 + 1024);
            dma4sc((lane16 >> 2) + (unsigned)(cw * 256), ws32, OFF_NB + cw * 256);
        }
        if (cw == 3) dma4sc(tag_addr(lane, d.unit, nq, st, xcd), ws32, OFF_TAG);
        if ((cw ^ 2) + nq == 0 && lane < 32) st16sc(lane16, (char*)ws32 + WS_SUM + st * 512, u32x4{0u, 0u, 0u, 0u});
        SB
        if (SD) __builtin_amdgcn_s_sleep(SD);
        if (SF && EB > 0) { prepB<0>(d); asm volatile("s_nop 0"); issueB<0>(d); issueB<1>(d); issueB<2>(d); issueB<3>(d); }
        if (EB > 1 && (EB > 2 || nq != 3)) { prepB<1>(d); asm volatile("s_nop 0"); issueB<0>(d); issueB<1>(d); issueB<2>(d); issueB<3>(d); }
        SB
    } else {
        Cross& c = x;
        srcA = (unsigned)(((lane & 7) ^ g) * 16); srcB = srcA ^ 64u;
        unsigned ib0 = (unsigned)(OFF_IDX + cw * 2048 + (lane >> 3) * 104);
#pragma unroll
        for (int k = 0; k < 4; ++k) { ib[k] = ib0 + (unsigned)(((k + rho) & 3) * 8); PIN(ib[k]); c.tb[k] = (const char*)table - k * 1024; PINS(c.tb[k]); }
        PIN(srcA); PIN(srcB); PIN(ib0);
        set_m0<0>(c.ldsb);
        SB
        WAITCNT(0, 15);
        asm volatile("" ::: "memory");
        STAMP(1)
        TP(c.tq[0])
        ia[0] = LDS(const u32x2, ib0 + 96); ic[0] = LDS(const u32x2, ib0 + 96 + 832);
#pragma unroll
        for (int q = 1; q < 3; ++q) { ia[q] = LDS(const u32x2, ib[q - 1]); ic[q] = LDS(const u32x2, ib[q - 1] + 832); }
        SB
#define GATHER(q) issueA<0>(c, ia[q][0] * 128u + srcA); issueA<1>(c, ic[q][0] * 128u + srcB); issueA<2>(c, ia[q][1] * 128u + srcA); issueA<3>(c, ic[q][1] * 128u + srcB);
        GATHER(0)
        set_m0<SLOT>(c.ldsb);
        SB
        GATHER(1)
        set_m0<2 * SLOT>(c.ldsb);
        TP(c.tq[1])
    }
    TS(tz3)
    int crs = deep ^ 1;
    PINS(crs); PINS(deep);
    if (crs == 0) {
        Deep& c = d;
        int lane;
        asm volatile("v_mbcnt_lo_u32_b32 %0, -1, 0\n\tv_mbcnt_hi_u32_b32 %0, -1, %0" : "=v"(lane));
        const int l15 = lane & 15, g = lane >> 4, tid = w * 64 + lane;
        c.c = cw; c.nq = nq; c.rho = rho; c.st = st; c.lane16 = (unsigned)lane * 16u; c.ws = (char*)ws32;
        c.m3 = (unsigned)(cw ^ 3); c.m4 = c.m3 | min(c.m1, (unsigned)nq);
        c.tad = tag_addr(lane, c.unit, nq, st, xcd);
        PIN(c.tad);
        SB
        mkboff(d, lane, cw, nq);
        {
            const unsigned a0 = (unsigned)(cw * 4096 + (cw & 1) * 2048 + l15 * 128 + ((4 * (cw >> 1) + g) ^ (l15 >> 1)) * 16);
            const int q = l15 >> 2, p = l15 & 3;
            const unsigned b0 = (unsigned)(OFF_B + cw * 4096 + (8 * g + q) * 128 + ((p >> 1) ^ (q & 2) ^ ((g & 1) << 2)) * 16 + (p & 1) * 8);
#pragma unroll
            for (int k = 0; k < 4; ++k) { c.ra[k] = a0 ^ (unsigned)(k * 4096); c.rb[k] = b0 ^ (unsigned)(k * 32); PIN(c.ra[k]); PIN(c.rb[k]); }
#pragma unroll
            for (int k = 0; k < 16; ++k) { c.ac[k >> 2][k & 3] = f32x4{0.f, 0.f, 0.f, 0.f}; PIN(c.ac[k >> 2][k & 3]); }
        }
        unsigned exw[4], fl = (c.lane16 >> 2) + (unsigned)(OFF_FL + cw * 256), fr = c.lane16 + OFF_FL, s2 = c.lane16 >> 3, s8 = c.lane16 >> 1;
#pragma unroll
        for (int k = 0; k < 4; ++k) { exw[k] = c.lane16 + (unsigned)(OFF_B + (cw ^ k) * 4096); PIN(exw[k]); }
        double big = 0x1.8p52;
        float va[2] = {0.f, 0.f};
        const unsigned dl = (unsigned)(l15 - 4 * g), on = (dl & 1u) ? 0x3f800000u : 0x3f80u;
        u32x4 unit = {dl < 2u ? on : 0u, dl - 2u < 2u ? on : 0u, dl < 2u ? on : 0u, dl - 2u < 2u ? on : 0u};
        PIN(fl); PIN(fr); PIN(s2); PIN(s8); PIN(big); PIN(gc0); PIN(gc1); PIN(va[0]); PIN(va[1]); PIN(unit);
        c.wsf = c.ws + WS_FRAG + nq * 65536 + cw * 4096;
        c.pmask = cw != 3 ? 0ull : (c.m1 == 0u ? 0x1ffull : 0x100ull);
        PINS(c.pmask); PINS(c.m3); PINS(c.m4); PINS(c.wsf);
        if (EB < 1) { prepB<0>(c); asm volatile("s_nop 0"); issueB<0>(c); issueB<1>(c); issueB<2>(c); issueB<3>(c); }
        if (EB < 2 || (EB == 2 && nq == 3)) { prepB<1>(c); asm volatile("s_nop 0"); issueB<0>(c); issueB<1>(c); issueB<2>(c); issueB<3>(c); }
        TP(c.tq[0])
        WAITCNT(SW, 0); BARRIER;
        TP(c.tq[1])
        prepB<2>(c); asm volatile("s_nop 0"); issueB<0>(c); issueB<1>(c); issueB<2>(c); issueB<3>(c);
        TP(c.tq[2])
        produce(c);
        if (SW > 4) WAITX(8, 2, c.m1)
        c.tag = LDSV(OFF_TAG + (c.lane16 >> 2));
        PIN(c.tag);
        TP(c.tq[3])
        BARRIER;
        TP(c.tq[4])
        gp sums = (gp)c.ws + (unsigned)(WS_SUM + (s0 + 16 * cw) * 8), outp = out + (unsigned)((s0 + 16 * cw) * 2);
        PINS(sums); PINS(outp);
        DStep<0>::run(c);
        TS(c.ts[13])
        u32x4 px[3][2];
#define EXC(e) { _Pragma("unroll") for (int k = 0; k < 8; ++k) px[(e) - 1][k >> 2][k & 3] = cvt_o(c.ac[e][k >> 1][2 * (k & 1)], c.ac[e][k >> 1][2 * (k & 1) + 1]); SB }
#define EXA(e, k) (((e) == 3 ? 3 * SLOT : ((e) - 1) * 2048) + (k) * 1024)
#define EXW(e) { LDS(u32x4, exw[0] + EXA(e, 0)) = px[(e) - 1][0]; LDS(u32x4, exw[0] + EXA(e, 1)) = px[(e) - 1][1]; SB }
#define EX2(e, k) { px[(e) - 1][(k) >> 1][2 * ((k) & 1)] = cvt_o(c.ac[e][k][0], c.ac[e][k][1]); px[(e) - 1][(k) >> 1][2 * ((k) & 1) + 1] = cvt_o(c.ac[e][k][2], c.ac[e][k][3]); SB }
        WAITCNT(0, 15); SB
        LDS(unsigned, fl + 2048) = fl; SB
        TE(c.tq[0])
        m4<12, 3>(c); m4<12, 2>(c);
        mk<12, 1, 0>(c); mk<12, 1, 1>(c); EX2(3, 0) mk<12, 1, 2>(c); EX2(3, 1) mk<12, 1, 3>(c); EX2(3, 2)
        mk<12, 0, 0>(c); EX2(3, 3) mk<12, 0, 1>(c); EX2(2, 0) mk<12, 0, 2>(c); EX2(2, 1) mk<12, 0, 3>(c); EX2(2, 2)
        EX2(2, 3) EXC(1)
        EXW(3) EXW(2) EXW(1)
        TE(c.tq[1])
        SB
        LDS(unsigned, fl + 1024) = fl;
        SB
        unsigned long long watch = 0ull;
        if (cw == 3) {
            watch = __builtin_amdgcn_ballot_w64(LDSV(OFF_TAG + 512 + (c.lane16 >> 2)) == c.tag) & (nq ? 0ull : 0xe00ull);
            if (c.m1 == 0u) {
                const u32x4 e4 = LDS(const u32x4, OFF_NB + c.lane16), l4 = LDS(const u32x4, OFF_NB + 1024 + c.lane16);
                watch |= __builtin_amdgcn_ballot_w64(e4[0] == l4[0] || e4[1] == l4[1] || e4[2] == l4[2] || e4[3] == l4[3]);
            }
            publish(c, c.tag + 2u);
        }
        TE(c.tq[2])
#pragma unroll 1
        for (int it = 0; it < (1 << 20) && __builtin_amdgcn_ballot_w64(LDSV(fr + 1024) == 0u) != 0ull; ++it) { }
        asm volatile("" ::: "memory");
        TE(c.tq[3])
        u32x2 pa[4], pb[4];
#pragma unroll
        for (int k = 0; k < 2; ++k) px[2][k] = LDS(const u32x4, exw[3] + EXA(3, k));
#pragma unroll
        for (int k = 0; k < 4; ++k) {
            pa[k] = *(volatile const __attribute__((address_space(3))) u32x2*)(size_t)(exw[1] + EXA(1, k >> 1) + (k & 1) * 8);
            pb[k] = *(volatile const __attribute__((address_space(3))) u32x2*)(size_t)(exw[2] + EXA(2, k >> 1) + (k & 1) * 8);
        }
        SB
#pragma unroll
        for (int k = 0; k < 4; ++k)
            asm volatile("v_mfma_f32_16x16x16_bf16 %0, %1, %2, %0" : "+v"(c.ac[0][k]) : "v"(u32x2{unit[0], unit[1]}), "v"(u32x2{px[2][k >> 1][2 * (k & 1)], px[2][k >> 1][2 * (k & 1) + 1]}));
        asm volatile("s_nop 2");
#pragma unroll
        for (int k = 0; k < 4; ++k) mfa(c.ac[0][k], unit, u32x4{pa[k][0], pa[k][1], pb[k][0], pb[k][1]});
        asm volatile("s_nop 4");
#pragma unroll
        for (int k = 0; k < 4; ++k) {
            float x[4] = {c.ac[0][k][0], c.ac[0][k][1], c.ac[0][k][2], c.ac[0][k][3]}, y[4];
            asm volatile("v_mul_f32 %4, %0, %0\n\tv_mul_f32 %5, %1, %1\n\tv_mul_f32 %6, %2, %2\n\tv_mul_f32 %7, %3, %3\n\t"
                         "v_fma_f32 %4, %4, %15, %14\n\tv_fma_f32 %5, %5, %15, %14\n\tv_fma_f32 %6, %6, %15, %14\n\tv_fma_f32 %7, %7, %15, %14\n\t"
                         "v_mul_f32 %4, %4, %0\n\tv_mul_f32 %5, %5, %1\n\tv_mul_f32 %6, %6, %2\n\tv_mul_f32 %7, %7, %3\n\t"
                         "v_exp_f32 %4, %4\n\tv_exp_f32 %5, %5\n\tv_exp_f32 %6, %6\n\tv_exp_f32 %7, %7\n\t"
                         "v_mul_f32 %0, %0, %10\n\tv_mul_f32 %1, %1, %11\n\tv_mul_f32 %2, %2, %12\n\tv_mul_f32 %3, %3, %13\n\t"
                         "v_add_f32 %4, 1.0, %4\n\tv_add_f32 %5, 1.0, %5\n\tv_add_f32 %6, 1.0, %6\n\tv_add_f32 %7, 1.0, %7\n\t"
                         "v_rcp_f32 %4, %4\n\tv_rcp_f32 %5, %5\n\tv_rcp_f32 %6, %6\n\tv_rcp_f32 %7, %7\n\t"
                         "v_fmac_f32 %8, %0, %4\n\tv_fmac_f32 %9, %1, %5\n\tv_fmac_f32 %8, %2, %6\n\tv_fmac_f32 %9, %3, %7"
                         : "+v"(x[0]), "+v"(x[1]), "+v"(x[2]), "+v"(x[3]), "=&v"(y[0]), "=&v"(y[1]), "=&v"(y[2]), "=&v"(y[3]), "+v"(va[0]), "+v"(va[1])
                         : "v"(c.w2[4 * k]), "v"(c.w2[4 * k + 1]), "v"(c.w2[4 * k + 2]), "v"(c.w2[4 * k + 3]), "v"(gc0), "v"(gc1));
        }
        TE(c.tq[4])
        const float v = sum16(sum32(va[0] + va[1]));
        TS(c.ts[14])
        if (__builtin_amdgcn_inverse_ballot_w64(0xffffull)) deliver(c, v, 2 * nq, s2, s8, big, sums, outp);
        if (__builtin_expect(watch != 0ull, 0)) {
            const u32x4 e4 = LDS(const u32x4, OFF_NB + c.lane16), l4 = LDS(const u32x4, OFF_NB + 1024 + c.lane16);
            bool opn[4];
#pragma unroll
            for (int k = 0; k < 4; ++k) opn[k] = c.m1 == 0u && e4[k] == l4[k];
            bool own = nq == 0 && lane >= 9 && lane < 12 && LDSV(OFF_TAG + 512 + lane * 4) == c.tag;
            unsigned f = c.tag + 3u;
#pragma unroll 1
            for (int it = 0; it < (1 << 22) && __builtin_amdgcn_ballot_w64(opn[0] || opn[1] || opn[2] || opn[3] || own) != 0ull; ++it) {
                bool chg = false;
#pragma unroll
                for (int k = 0; k < 4; ++k)
                    if (opn[k] && __hip_atomic_load(ws32 + lane * 4 + k, __ATOMIC_RELAXED, __HIP_MEMORY_SCOPE_AGENT) != e4[k]) { opn[k] = false; chg = true; }
                if (own && __hip_atomic_load(ws32 + (c.tad >> 2), __ATOMIC_RELAXED, __HIP_MEMORY_SCOPE_AGENT) != c.tag) { own = false; chg = true; }
                if (__builtin_amdgcn_ballot_w64(chg) != 0ull) { publish(c, f); ++f; }
                else __builtin_amdgcn_s_sleep(2);
            }
        }
        FINISH(c)
    } else {
        Cross& c = x;
#pragma unroll
        for (int q = 3; q < NP; ++q) { ia[q] = LDS(const u32x2, ib[(q - 1) & 3] + ((q - 1) >> 2) * 32); ic[q] = LDS(const u32x2, ib[(q - 1) & 3] + ((q - 1) >> 2) * 32 + 832); }
#pragma unroll
        for (int q = 3; q < NP; ++q) {
            c.ga[4 * q - 12] = ia[q][0] * 128u + srcA; c.ga[4 * q - 11] = ic[q][0] * 128u + srcB;
            c.ga[4 * q - 10] = ia[q][1] * 128u + srcA; c.ga[4 * q - 9] = ic[q][1] * 128u + srcB;
        }
#pragma unroll
        for (int k = 0; k < 40; ++k) PIN(c.ga[k]);
        {
            const int G = (g + rho) & 3;
            const unsigned ea = (unsigned)(cw * 4096 + l15 * 128);
            c.re[0] = ea + (unsigned)((g ^ (l15 >> 1)) * 16); c.re[1] = c.re[0] ^ 64u;
            c.rx[0] = ea + (unsigned)(((g + 1) & 3) * SLOT + ((2 * nq) ^ (l15 >> 1)) * 16); c.rx[1] = c.rx[0] ^ 16u;
            c.rx0 = ea + (unsigned)((G & 1) * 2048 + ((2 * nq + (G >> 1)) ^ (l15 >> 1)) * 16);
            c.six = (l15 & 1) ? 0xeeeeu : 0x4444u;
#pragma unroll
            for (int k = 0; k < 4; ++k) { const unsigned o = (l15 >> 1) == 2 * k + (g >> 1) ? 0x3f803f80u : 0u; c.on[k] = u32x4{o, o, o, o}; PIN(c.on[k]); }
            PIN(c.re[0]); PIN(c.re[1]); PIN(c.rx[0]); PIN(c.rx[1]); PIN(c.rx0); PIN(c.six);
            zclear<3>(c); zclear<0>(c);
        }
        SB
        BARRIER;
        GATHER(2)
        TP(c.tq[2])
        {
            const int q = (lane >> 2) & 3, p = lane & 3;
            const unsigned a0 = (unsigned)(OFF_WBI + (8 * g + q) * 32 + p * 8), a1 = (unsigned)(OFF_WBI + (8 * g + q) * 32 + ((p + rho) & 3) * 8);
#pragma unroll
            for (int k = 0; k < 2; ++k) {
                c.wb[k] = CAT8(tr_read(a0 + k * 1024), tr_read(a0 + k * 1024 + 128));
                c.wr[k] = CAT8(tr_read(a1 + k * 1024), tr_read(a1 + k * 1024 + 128));
            }
#pragma unroll
            for (int k = 0; k < 2; ++k) { PIN(c.wb[k]); PIN(c.wr[k]); }
        }
        gp sums = (gp)c.ws + (unsigned)(WS_SUM + (s0 + 16 * cw) * 8), outp = out + (unsigned)((s0 + 16 * cw) * 2);
        PINS(sums); PINS(outp);
        c.wsf = c.ws + WS_FRAG + 32768 + nq * 65536 + cw * 4096; PINS(c.wsf);
        TP(c.tq[3])
        WAITCNT(8, 0); BARRIER;
        TP(c.tq[4])
        __builtin_amdgcn_s_setprio(1);
        CStep<0>::run(c);
        TS(c.ts[11])
        u32x4 bq[4];
        u32x4 (&bp)[4] = c.bp;
        f32x2 T[6][8];
#define SPLIT(s0, s1, i0, i1) { _Pragma("unroll") for (int s = s0; s < s1; ++s) _Pragma("unroll") for (int i_ = i0; i_ < i1; ++i_) { \
            const f32x4 x = c.ew[2 * (1 + 4 * (s >> 1) + (i_ >> 1)) + (s & 1)]; T[s][i_] = f32x2{x[2 * (i_ & 1)], x[2 * (i_ & 1) + 1]}; } }
#define CB(B, i, h, k) { B[(2 * (i) + (h)) & 3][k] = cvt_o(T[2 * (k)][i][h], T[2 * (k) + 1][i][h]); }
#define SC(i, k) { asm volatile("v_pk_mul_f32 %0, %1, %0 op_sel_hi:[0,1]" : "+v"(T[2 * (k)][i]) : "v"(ao[k])); asm volatile("v_pk_mul_f32 %0, %1, %0 op_sel:[1,0]" : "+v"(T[2 * (k) + 1][i]) : "v"(ao[k])); }
        SPLIT(0, 4, 0, 8)
        SB
#define FPOS 2
#define FI(n) if ((n) >= FPOS && (n) < FPOS + 4) { if (!(((n) - FPOS) & 1)) { set_m0<FOFF[2 + ((((n) - FPOS) >> 1) & 1)]>(c.ldsb); asm volatile("s_nop 0"); } \
            dma16sco<(((n) - FPOS) & 1) * 2048>(c.lane16, c.wsf + (((n) - FPOS) >> 1) * 16384); dma16sco<(((n) - FPOS) & 1) * 2048 + 1024>(c.lane16, c.wsf + (((n) - FPOS) >> 1) * 16384); SB }
        cmm<10, 2>(c); rdE<11, 0>(c);
        cmm<10, 3>(c); rdE<11, 1>(c);
        cmm<10, 4>(c); rdE<11, 2>(c);
        cmm<10, 5>(c); rdE<11, 3>(c);
        cmm<10, 0>(c); rdE<12, 0>(c); rdE<12, 1>(c);
        cmm<10, 1>(c); rdE<12, 2>(c); rdE<12, 3>(c);
        FI(0)
        CB(bp, 0, 0, 1) CB(bp, 0, 1, 1) CB(bp, 1, 0, 1) CB(bp, 1, 1, 1)
        cmm<11, 0>(c); sw1<2, 0, 0>(c);
        cmm<11, 1>(c); sw1<2, 0, 1>(c);
        cmm<12, 0>(c); sw1<2, 0, 2>(c);
        cmm<12, 1>(c); sw1<2, 0, 3>(c);
        STAMP(2)
        TS(c.ts[12])
        cmm<11, 2>(c); sw1<2, 0, 4>(c);
        cmm<11, 3>(c); sw1<2, 0, 5>(c);
        cmm<11, 4>(c); sw1<2, 0, 6>(c);
        cmm<11, 5>(c); zpack<2>(c);
        cmm<12, 2>(c); sw1<2, 0, 7>(c);
        const f32x4 hp = mf0(c.s1, c.zq);
        cmm<12, 3>(c); cmm<12, 4>(c); cmm<12, 5>(c);
        rdX<12>(c);
        FI(1)
        float hq[4];
        asm volatile("v_mul_f32 %0, %4, %4\n\tv_mul_f32 %1, %5, %5\n\tv_mul_f32 %2, %6, %6\n\tv_mul_f32 %3, %7, %7\n\t"
                     "v_fma_f32 %0, %0, %9, %8\n\tv_fma_f32 %1, %1, %9, %8\n\tv_fma_f32 %2, %2, %9, %8\n\tv_fma_f32 %3, %3, %9, %8\n\t"
                     "v_mul_f32 %0, %0, %4\n\tv_mul_f32 %1, %1, %5\n\tv_mul_f32 %2, %2, %6\n\tv_mul_f32 %3, %3, %7\n\t"
                     "v_exp_f32 %0, %0\n\tv_exp_f32 %1, %1\n\tv_exp_f32 %2, %2\n\tv_exp_f32 %3, %3\n\t"
                     "v_fma_f32 %0, %0, %10, %10\n\tv_fma_f32 %1, %1, %10, %10\n\tv_fma_f32 %2, %2, %10, %10\n\tv_fma_f32 %3, %3, %10, %10\n\t"
                     "v_rcp_f32 %0, %0\n\tv_rcp_f32 %1, %1\n\tv_rcp_f32 %2, %2\n\tv_rcp_f32 %3, %3\n\t"
                     "v_mul_f32 %0, %0, %4\n\tv_mul_f32 %1, %1, %5\n\tv_mul_f32 %2, %2, %6\n\tv_mul_f32 %3, %3, %7\n\t"
                     "v_cvt_pk_bf16_f32 %0, %0, %1\n\tv_cvt_pk_bf16_f32 %1, %2, %3"
                     : "=&v"(hq[0]), "=&v"(hq[1]), "=&v"(hq[2]), "=&v"(hq[3]) : "v"(hp[0]), "v"(hp[1]), "v"(hp[2]), "v"(hp[3]), "v"(c.k0), "v"(c.k1), "v"(c.k2));
        FI(2)
        const f32x2 hb = {hq[0], hq[1]};
        swn<2, 1, 0, 2>(c);
        f32x4 av[2];
        asm volatile("v_mfma_f32_16x16x16_bf16 %0, %1, %2, 0" : "=&v"(av[0]) : "v"(c.wa[0]), "v"(hb));
        asm volatile("v_mfma_f32_16x16x16_bf16 %0, %1, %2, 0" : "=&v"(av[1]) : "v"(c.wa[1]), "v"(hb));
        FI(3)
        swn<2, 1, 2, 8>(c);
        swn<2, 2, 0, 2>(c);
        FI(4)
        TS(c.ts[13])
        SB
        float as[8];
#pragma unroll
        for (int r = 0; r < 8; ++r) as[r] = exp_o(av[r >> 2][r & 3]);
        asm volatile("v_add_f32 %0, 1.0, %0\n\tv_add_f32 %1, 1.0, %1\n\tv_add_f32 %2, 1.0, %2\n\tv_add_f32 %3, 1.0, %3\n\tv_add_f32 %4, 1.0, %4\n\tv_add_f32 %5, 1.0, %5\n\tv_add_f32 %6, 1.0, %6\n\tv_add_f32 %7, 1.0, %7\n\t"
                     "v_rcp_f32 %0, %0\n\tv_rcp_f32 %1, %1\n\tv_rcp_f32 %2, %2\n\tv_rcp_f32 %3, %3\n\tv_rcp_f32 %4, %4\n\tv_rcp_f32 %5, %5\n\tv_rcp_f32 %6, %6\n\tv_rcp_f32 %7, %7"
                     : "+v"(as[0]), "+v"(as[1]), "+v"(as[2]), "+v"(as[3]), "+v"(as[4]), "+v"(as[5]), "+v"(as[6]), "+v"(as[7]));
        FI(5)
        unsigned flg = LDSV(c.wf[1] + (OFF_FL + 2048 - OFF_B - SLOT));
        SB
        swn<2, 2, 2, 8>(c);
        FI(6)
        SPLIT(4, 6, 0, 2) SPLIT(4, 6, 4, 6)
        TE(c.tq[0])
        SB
        WAITCNT(63, 0);
        wait_set(flg, c.wf[1] + (OFF_FL + 2048 - OFF_B - SLOT));
        SB
#define WSEQ(n) LDS(const u32x4, c.wf[(((n) >> 4) & 1) ^ 1] + ((n) >> 5) * SLOT + ((n) & 15) * 1024)
        u32x4 wq[64];
#pragma unroll
        for (int r = 0; r < WPRE; ++r) wq[r] = WSEQ(r);
        SB
        CB(bp, 0, 0, 2) CB(bp, 0, 1, 2)
        f32x2 ao[4] = {{as[0], as[1]}, {as[2], as[3]}, {as[4], as[5]}, {as[6], as[7]}};
        {
            f32x2 m[4] = {{c.ew[0][0], c.ew[0][1]}, {c.ew[1][0], c.ew[1][1]}, {c.ew[0][2], c.ew[0][3]}, {c.ew[1][2], c.ew[1][3]}};
#pragma unroll
            for (int r = 0; r < 4; r += 2) {
                asm volatile("v_pk_mul_f32 %0, %1, %0 op_sel_hi:[0,1]" : "+v"(m[r]) : "v"(ao[3]));
                asm volatile("v_pk_mul_f32 %0, %1, %0 op_sel:[1,0]" : "+v"(m[r + 1]) : "v"(ao[3]));
            }
            SC(0, 0) SC(0, 1) SC(0, 2)
#pragma unroll
            for (int r = 0; r < 4; ++r) bq[r][3] = cvt_o(m[r & 2][r & 1], m[(r & 2) + 1][r & 1]);
        }
        ao[3][0] = sel(c.am, as[7], as[6]);
        PIN(ao[3]);
        SB
        TE(c.tq[1])
        f32x4 dg;
        f32x4 u[2][4];
        f32x2 sv[2][4];
        u32x4 pk[7];
#define FM(h, tl) { _Pragma("unroll") for (int q = 0; q < 2; ++q) asm volatile("v_pk_fma_f32 %0, %1, %2, %3" : "=v"(sv[h][2 * (tl) + q]) : "v"(f32x2{u[h][2 + (tl)][2 * q], u[h][2 + (tl)][2 * q + 1]}), "v"(ao[2 * (tl) + q]), "v"(f32x2{u[h][tl][2 * q], u[h][tl][2 * q + 1]})); }
#define PK(i, s) { pk[s][(i) & 3] = cvt_o(sv[0][(s) >> 1][(s) & 1], sv[1][(s) >> 1][(s) & 1]); }
#define DOT(eh, s) { const u32x4 x_ = (s) < 6 ? c.X[1 + 4 * ((s) >> 1) + ((s) & 1) + 2 * (eh)] : c.X[0]; if ((eh) || (s)) mfa(dg, pk[s], x_); else dg = mf0(pk[s], x_); }
#pragma unroll
        for (int i = 0; i < 8; ++i) {
            if (i == 3) { SB wait_set(flg, c.wf[1] + (OFF_FL - OFF_B - SLOT)); SB }
            if (i == 1) { SB WAITCNT(0, 15); LDS(unsigned, (c.lane16 >> 2) + (unsigned)(OFF_FL + cw * 256)) = c.wf[0]; SB }
            if (i == 2) { TE(c.tq[2]) }
            if (i == 4) { TE(c.tq[3]) }
            if (i == 1) { SPLIT(4, 6, 2, 4) SPLIT(4, 6, 6, 8) }
#pragma unroll
            for (int j = 0; j < 8; ++j) {
                const int n = 8 * i + j, h = j >> 2;
                const bool nx = i < 7, pv = i >= 1;
                SB
                if (!(j & 1)) { if (n + WPRE <= 64) WAITCNT(63, WPRE - 2); SB }
                u[h][j & 3] = mf0(wq[n], (j & 2) ? bq[(2 * i + h) & 3] : bp[(2 * i + h) & 3]);
                SB
                if (n == 17) flg = LDSV(c.wf[1] + (OFF_FL - OFF_B - SLOT));
                if (n + WPRE < 64) wq[n + WPRE] = WSEQ(n + WPRE);
                SB
                if (i == 0) { if (j == 2) swn<2, 3, 0, 1>(c); if (j == 3) swn<2, 3, 1, 2>(c); if (j == 4) swn<2, 3, 2, 4>(c); if (j == 5) swn<2, 3, 4, 5>(c); if (j == 6) swn<2, 3, 5, 7>(c); if (j == 7) swn<2, 3, 7, 8>(c); }
                if (j == 0) { CB(bq, i, 0, 0) if (i == 5) DOT(0, 4) CB(bq, i, 0, 1) }
                if (j == 1) { CB(bq, i, 0, 2) if (i == 5) DOT(0, 5) CB(bq, i, 1, 0) CB(bq, i, 1, 1) }
                if (j == 2) { CB(bq, i, 1, 2) if (i == 5) DOT(0, 6) if (pv) PK(i - 1, 0) }
                if (j == 3) { if (pv) { PK(i - 1, 1) if (i == 4) DOT(0, 0) PK(i - 1, 2) } }
                if (j == 4) { if (pv) { PK(i - 1, 3) if (i == 4) DOT(0, 1) PK(i - 1, 4) } }
                if (j == 5) { if (pv) { PK(i - 1, 5) if (i == 4) DOT(0, 2) PK(i - 1, 6) } }
                if (j == 6) { if (nx && i) { CB(bp, i + 1, 0, 0) if (i == 4) DOT(0, 3) CB(bp, i + 1, 1, 0) } }
                if (j == 7) {
                    if (nx && i) { CB(bp, i + 1, 0, 1) CB(bp, i + 1, 1, 1) }
                    if (nx) { CB(bp, i + 1, 0, 2) CB(bp, i + 1, 1, 2) }
                    FM(0, 0) FM(0, 1)
                    if (nx) { SC(i + 1, 0) SC(i + 1, 1) SC(i + 1, 2) } else asm volatile("s_nop 3");
                    FM(1, 0) FM(1, 1)
                }
            }
        }
        PK(7, 0) PK(7, 1) PK(7, 2) DOT(1, 0) PK(7, 3) PK(7, 4) DOT(1, 1) PK(7, 5) PK(7, 6) DOT(1, 2)
        unsigned s2;
        asm volatile("v_mbcnt_lo_u32_b32 %0, -1, 0\n\tv_mbcnt_hi_u32_b32 %0, -1, %0" : "=v"(s2));
        DOT(1, 3)
        asm volatile("v_lshlrev_b32 %0, 1, %0\n\tv_and_b32 %0, 30, %0" : "+v"(s2));
        DOT(1, 4)
        unsigned s8 = s2 * 4u;
        double big = 0x1.8p52;
        PIN(s8); PIN(big);
        SB
        DOT(1, 5) DOT(1, 6)
        float v, v2;
        asm volatile("s_mov_b64 vcc, %6\n\ts_nop 6\n\tv_cndmask_b32 %0, %2, %3, vcc\n\tv_cndmask_b32 %1, %4, %5, vcc\n\tv_cndmask_b32 %0, %0, %1, %7"
                     : "=&v"(v), "=&v"(v2) : "v"(dg[0]), "v"(dg[1]), "v"(dg[2]), "v"(dg[3]), "s"(0xaaaaaaaaaaaaaaaaull), "s"(0xccccccccccccccccull) : "vcc");
        TE(c.tq[4])
        STAMP(4)
        TS(c.ts[14])
        if (__builtin_amdgcn_inverse_ballot_w64(0xf0000f0000f0000full)) deliver(c, v, 2 * nq + 1, s2, s8, big, sums, outp);
        const int ln = __builtin_amdgcn_mbcnt_hi(~0u, __builtin_amdgcn_mbcnt_lo(~0u, 0u));
        const int lane = ln, tid = w * 64 + ln;
        (void)lane; (void)tid;
        FINISH(c)
    }
}

extern "C" void kernel_launch(void* const* d_in, const int* in_sizes, int n_in,
                              void* d_out, int out_size, void* d_ws, size_t ws_size,
                              hipStream_t stream) {
    const int* idx = (const int*)d_in[0];
    const uint16_t* table = (const uint16_t*)d_in[1];
    const uint16_t* se_w1 = (const uint16_t*)d_in[2];
    const uint16_t* se_w2 = (const uint16_t*)d_in[3];
    const uint16_t* w_bi = (const uint16_t*)d_in[4];
    const uint16_t* w_cross = (const uint16_t*)d_in[5];
    const uint16_t* w_deep1 = (const uint16_t*)d_in[6];
    const uint16_t* w_deep2 = (const uint16_t*)d_in[7];
    (void)hipFuncSetAttribute((const void*)k_main, hipFuncAttributeMaxDynamicSharedMemorySize, LDS_T);
    k_main<<<256, 512, LDS_T, stream>>>(idx, table, w_bi, w_deep1, w_cross, (unsigned*)d_ws, se_w1, se_w2, w_deep2, (__hip_bfloat16*)d_out, (unsigned long long*)((char*)d_ws + (128ull << 20)));
}
```

```cpp
#define PFC 143
#include <hip/hip_runtime.h>
#include <hip/hip_bf16.h>
#include <stdint.h>

#define NF 26
#define NP 13
#define PROD_CLASS(slot) ((((slot) & 3) == 1) && (((slot) >> 2) & 1) == 0)
#define STAMP(k)
#define TS(x)
#define TP(x)
#define TE(x) TS(x)
using bf16x8 = __attribute__((ext_vector_type(8))) short;
using f32x4 = __attribute__((ext_vector_type(4))) float;
using f32x2 = __attribute__((ext_vector_type(2))) float;
using s16x4 = __attribute__((ext_vector_type(4))) short;
using u32x4 = __attribute__((ext_vector_type(4))) unsigned;
using u32x8 = __attribute__((ext_vector_type(8))) unsigned;
using u32x2 = __attribute__((ext_vector_type(2))) unsigned;
using bf2 = __attribute__((ext_vector_type(2))) __bf16;
#define PIN(x) asm volatile("" : "+v"(x))
#define PINS(x) asm volatile("" : "+s"(x))
#define SB __builtin_amdgcn_sched_barrier(0);
#define CAT8(a, b) __builtin_shufflevector((a), (b), 0, 1, 2, 3, 4, 5, 6, 7)

#define SLOT 16384
#define OFF_B 65536
#define OFF_FL 131072
#define OFF_WBI (OFF_FL + 4096)
#define OFF_SW1 (OFF_WBI + 2048)
#define OFF_SW2 (OFF_SW1 + 1024)
#define OFF_WD2 (OFF_SW2 + 1024)
#define OFF_TAG (OFF_WD2 + 256)
#define OFF_NB (OFF_TAG + 768)
#define OFF_JNK (OFF_NB + 2048)
#define OFF_IDX (OFF_JNK + 2048)
#define OFF_RAW (OFF_IDX + 8192)
#define LDS_T (OFF_IDX + 16384)
constexpr int FOFF[4] = {OFF_B + SLOT, SLOT, OFF_B + 2 * SLOT, 2 * SLOT};
#define P_FLAG 3
#define P_POLL 6
#define P_CHK (P_POLL + 3)
#define P_STAT 9

#define WS_FLAG 1024
#define WS_SUM 4096
#define WS_SIDE 65536
#define WS_FRAG (1 << 20)

#define LDS(T, a) (*(__attribute__((address_space(3))) T*)(size_t)(a))
#define LDSV(a) (*(volatile const __attribute__((address_space(3))) unsigned*)(size_t)(a))
__device__ __forceinline__ unsigned pk_bf16(float lo, float hi) {
    const f32x2 v = {lo, hi};
    return __builtin_bit_cast(unsigned, __builtin_convertvector(v, bf2));
}
__device__ __forceinline__ unsigned cvt_o(float lo, float hi) { unsigned r; asm volatile("v_cvt_pk_bf16_f32 %0, %1, %2" : "=v"(r) : "v"(lo), "v"(hi)); return r; }
__device__ __forceinline__ float exp_o(float a) { float r; asm volatile("v_exp_f32 %0, %1" : "=v"(r) : "v"(a)); return r; }
template <typename A, typename B> __device__ __forceinline__ f32x4 mf0(const A& a, const B& b) { f32x4 d; asm volatile("v_mfma_f32_16x16x32_bf16 %0, %1, %2, 0" : "=v"(d) : "v"(a), "v"(b)); return d; }
template <typename A, typename B> __device__ __forceinline__ void mfa(f32x4& d, const A& a, const B& b) { asm volatile("v_mfma_f32_16x16x32_bf16 %0, %1, %2, %0" : "+v"(d) : "v"(a), "v"(b)); }
__device__ __forceinline__ float lo_f(unsigned x) { return __uint_as_float(x << 16); }
__device__ __forceinline__ float hi_f(unsigned x) { return __uint_as_float(x & 0xffff0000u); }
__device__ __forceinline__ s16x4 tr_read(unsigned a) {
    typedef short v4s __attribute__((ext_vector_type(4)));
    return __builtin_amdgcn_ds_read_tr16_b64_v4i16((__attribute__((address_space(3))) v4s*)(size_t)a);
}
#define DMA(name, ins) __device__ __forceinline__ void name(unsigned voff, const void* sbase, unsigned ldsaddr) { \
    asm volatile("s_mov_b32 m0, %2\n\ts_nop 0\n\t" ins :: "v"(voff), "s"(sbase), "s"(ldsaddr) : "memory"); }
DMA(dma16, "global_load_lds_dwordx4 %0, %1") DMA(dma4, "global_load_lds_dword %0, %1")
DMA(dma16sc, "global_load_lds_dwordx4 %0, %1 sc1") DMA(dma4sc, "global_load_lds_dword %0, %1 sc1")
__device__ __forceinline__ void st16sc(unsigned voff, void* sbase, u32x4 v) {
    asm volatile("global_store_dwordx4 %0, %1, %2 sc1\n\ts_nop 1" :: "v"(voff), "v"(v), "s"(sbase) : "memory");
}
template <int O> __device__ __forceinline__ void set_m0(unsigned base) { asm volatile("s_add_u32 m0, %0, %1" :: "s"(base), "n"(O) : "memory", "scc"); }
template <int O> __device__ __forceinline__ void bdma16o(unsigned voff, __amdgpu_buffer_rsrc_t rs, unsigned soff) {
    asm volatile("buffer_load_dwordx4 %0, %1, %2 offen offset:%3 lds" :: "v"(voff), "s"(rs), "s"(soff), "n"(O) : "memory");
}
template <int O> __device__ __forceinline__ void dma16o(unsigned voff, const void* sbase) {
    asm volatile("global_load_lds_dwordx4 %0, %1 offset:%2" :: "v"(voff), "s"(sbase), "n"(O) : "memory");
}
template <int O> __device__ __forceinline__ void dma16sco(unsigned voff, const void* sbase) {
    asm volatile("global_load_lds_dwordx4 %0, %1 offset:%2 sc1" :: "v"(voff), "s"(sbase), "n"(O) : "memory");
}
#define WAITCNT(vm, lgkm) __builtin_amdgcn_s_waitcnt(((vm) & 15) | (((vm) >> 4) << 14) | 0x70 | ((lgkm) << 8))
#define WAITX(vm, x, m) { WAITCNT((vm) + (x), 0); asm volatile("s_cmp_eq_u32 %0, 0\n\ts_cbranch_scc1 1f\n\ts_waitcnt vmcnt(%1)\n1:" :: "s"(m), "n"(vm) : "memory", "scc"); }
#define BARRIER asm volatile("s_barrier" ::: "memory")
__device__ __forceinline__ float sum32(float v) { const u32x2 p = __builtin_amdgcn_permlane32_swap(__float_as_uint(v), __float_as_uint(v), false, false); return __uint_as_float(p[0]) + __uint_as_float(p[1]); }
__device__ __forceinline__ float sum16(float v) { const u32x2 p = __builtin_amdgcn_permlane16_swap(__float_as_uint(v), __float_as_uint(v), false, false); return __uint_as_float(p[0]) + __uint_as_float(p[1]); }
__device__ __forceinline__ float sel(unsigned m, float a, float b) {
    return __uint_as_float(__builtin_amdgcn_bitop3_b32(m, __float_as_uint(a), __float_as_uint(b), 0xca));
}
__device__ __forceinline__ void wait_set(unsigned f, unsigned a) {
    if (__builtin_amdgcn_ballot_w64(f == 0u) != 0ull) {
#pragma unroll 1
        for (int it = 0; it < (1 << 20) && __builtin_amdgcn_ballot_w64(LDSV(a) == 0u) != 0ull; ++it) { }
    }
    asm volatile("" ::: "memory");
}

struct Com {
    unsigned long long pa, pb;
    char* ws;
    unsigned lane16, ldsb;
    int c, nq, rho, st;
};
typedef __attribute__((address_space(1))) char* gp;
__device__ __forceinline__ void deliver(const Com& c, float v, int part, unsigned s2, unsigned s8, double big, gp sums, gp outp) {
    const gp side = (gp)c.ws + (WS_SIDE + c.st * 2048 + c.c * 64);
    u32x2 mine = __builtin_bit_cast(u32x2, __builtin_fma((double)v, 0x1p42, big));
    mine[1] += 0x04004000u - 0x43380000u;
    const unsigned ab = __float_as_uint(v) << 1;
    if (__builtin_expect(ab - 0x61000000u >= 0x22000000u && ab != 0u, 0)) {
        mine = u32x2{0u, 0x04004000u + (0x40000u << part)};
        __hip_atomic_store((__attribute__((address_space(1))) float*)(side + part * 256 + s2 * 2), v, __ATOMIC_RELAXED, __HIP_MEMORY_SCOPE_AGENT);
        asm volatile("s_waitcnt vmcnt(0)" ::: "memory");
    }
    const unsigned long long mv = __builtin_bit_cast(unsigned long long, mine);
    const unsigned long long tot = mv + __hip_atomic_fetch_add((__attribute__((address_space(1))) unsigned long long*)(sums + s8), mv, __ATOMIC_RELAXED, __HIP_MEMORY_SCOPE_AGENT);
    const unsigned th = (unsigned)(tot >> 32);
    if (th >= 0x20000000u) {
        const u32x2 dv = {(unsigned)tot, (th & 0x3ffffu) | 0x43300000u};
        float r = (float)__builtin_fma(__builtin_bit_cast(double, dv), 0x1p-42, -0x1.2p10);
        if (__builtin_expect(th >= 0x20040000u, 0)) {
#pragma unroll 1
            for (int k = 0; k < 8; ++k)
                if ((th >> (18 + k)) & 1u) r += __hip_atomic_load((__attribute__((address_space(1))) float*)(side + k * 256 + s2 * 2), __ATOMIC_RELAXED, __HIP_MEMORY_SCOPE_AGENT);
        }
        *(__attribute__((address_space(1))) uint16_t*)(outp + s2) = (uint16_t)pk_bf16(r, r);
    }
}

struct Deep : Com {
    __amdgpu_buffer_rsrc_t rw1;
    const char* wsf; const char* wd2;
    unsigned sG[4], sB;
    unsigned m1, m3, m4;
    int unit;
    unsigned long long pmask;
    unsigned tad, tag, pl;
    unsigned boff[4], ra[4], rb[4];
    float w2[16];
    f32x4 ac[4][4];
    bf16x8 fa[2][4];
    s16x4 lo[2][4], hi[2][4];
};
template <int Q> __device__ __forceinline__ void prepB(Deep& c) {
    if constexpr (Q == 0) c.sB = 24 * 32768; else c.sB = c.sG[(Q - 1) & 3] + ((Q - 1) >> 2) * (8 * 32768);
    PINS(c.sB);
    set_m0<OFF_B + (Q & 3) * SLOT>(c.ldsb);
}
template <int I> __device__ __forceinline__ void issueB(Deep& c) { bdma16o<I * 1024>(c.boff[I], c.rw1, c.sB); }
__device__ __forceinline__ void mkboff(Deep& d, int lane, int cw, int nq) {
    const unsigned b0 = (unsigned)(((cw & 1) * 64 + (cw >> 1) * 32 + (lane >> 3)) * 512 + nq * 128 + ((lane & 7) ^ ((lane >> 3) & 2)) * 16);
    d.boff[0] = b0; d.boff[1] = (b0 ^ 64u) + 3072; d.boff[2] = b0 + 6144; d.boff[3] = (b0 ^ 64u) + 9216;
#pragma unroll
    for (int k = 0; k < 4; ++k) PIN(d.boff[k]);
}
template <int P, int N> __device__ __forceinline__ void rd1(Deep& c) {
    constexpr int tab[12] = {0, 4, 5, 6, 7, 1, 2, 3, 8, 9, 10, 11}, k = tab[N < 12 ? N : 0];
    if constexpr (N >= 12) { }
    else if constexpr (k < 4) c.fa[P & 1][k] = LDS(const bf16x8, c.ra[k] + (P & 3) * SLOT);
    else if constexpr (k & 1) c.hi[P & 1][(k - 4) >> 1] = tr_read(c.rb[(k - 4) >> 1] + (P & 3) * SLOT + 512);
    else c.lo[P & 1][(k - 4) >> 1] = tr_read(c.rb[(k - 4) >> 1] + (P & 3) * SLOT);
    SB
}
template <int P, int M, int K> __device__ __forceinline__ void mk(Deep& c) {
    if constexpr (P >= 0) mfa(c.ac[M][K], CAT8(c.lo[P & 1][K], c.hi[P & 1][K]), c.fa[P & 1][M]);
    SB
}
template <int P, int N> __device__ __forceinline__ void m1(Deep& c) { mk<P, (N >> 1) & 3, (N >> 3) * 2 + (N & 1)>(c); }
template <int P, int M> __device__ __forceinline__ void m4(Deep& c) { mk<P, M, 0>(c); mk<P, M, 1>(c); mk<P, M, 2>(c); mk<P, M, 3>(c); }
#define LSTEP 0xbbaa
constexpr int lstep(int k) { return (LSTEP >> (4 * k)) & 15; }
constexpr int lfirst(int S) { int k = 0; while (k < 4 && lstep(k) < S) ++k; return k; }
constexpr int dops(int S) { return S < 8 ? 4 : 4 * (lfirst(S + 1) - lfirst(S)); }
static_assert(lstep(2) > P_CHK && lstep(3) > 10, "fragments: after the check, chunk 9 stays during step 10");
constexpr int dyoung(int S, int U) { return 4 * ((S < 8 ? S + 4 : 11 + lfirst(S + 1)) - (U + 1)) + (S == 1 || S == 2); }
template <int S, int I> __device__ __forceinline__ void dissue1(Deep& c) {
    constexpr int U = S < 8 ? S + 3 : 11 + lfirst(S) + (I >> 2);
    if constexpr (U < NP) {
        if constexpr ((I & 3) == 0) { prepB<U>(c); asm volatile("s_nop 0"); }
        issueB<I & 3>(c);
    } else {
        if constexpr ((I & 3) == 0) { set_m0<FOFF[U - NP]>(c.ldsb); asm volatile("s_nop 0"); }
        dma16sco<(I & 3) * 1024>(c.lane16, c.wsf + (U - NP) * 16384);
    }
    SB
}
template <int S, int I0, int I1> __device__ __forceinline__ void dissue(Deep& c) {
    if constexpr (I0 < I1 && I0 < dops(S)) { dissue1<S, I0>(c); dissue<S, I0 + 1, I1>(c); }
}
__device__ __forceinline__ unsigned tag_addr(int lane, int unit, int nq, int st, int xcd) {
    const unsigned l4 = (unsigned)lane * 4u;
    const unsigned a0 = (unsigned)(WS_FLAG + unit * 4) + l4 * 32u;
    const unsigned a1 = (((unsigned)(nq * 256) + l4 * 64u) & 768u) + (unsigned)(st * 4);
    const unsigned a2 = (unsigned)(WS_FLAG + xcd * 128 - 64) + l4;
    const unsigned lo = l4 < 32u ? a0 : a1, hi = l4 < 168u ? a2 : (unsigned)(st * 4);
    return l4 < 64u ? lo : hi;
}
__device__ __forceinline__ void publish(Deep& c, unsigned sv) {
    asm volatile("s_cmp_eq_u64 %3, 0\n\ts_cbranch_scc1 1f\n\ts_mov_b64 exec, %3\n\tglobal_store_dword %0, %1, %2 sc1\n\ts_mov_b64 exec, -1\n1:"
                 :: "v"(c.tad), "v"(sv), "s"(c.ws), "s"(c.pmask) : "memory", "scc");
}
__device__ __forceinline__ void produce(Deep& c) {
    if (c.m1 == 0u) {
        const int unit = c.unit;
        const unsigned p8 = (c.lane16 << 4) & 0x300u, e2 = (c.lane16 >> 5) & 0x1eu;
        const unsigned rb = (unsigned)(OFF_RAW + c.c * 32) + e2;
        const unsigned dm = p8 == (c.lane16 & 0x300u) ? ~0u : 0u;
        const int m = unit < 24 ? 2 * (unit & 6) + (unit & 1) + ((unit >> 2) & 2) : 4 * (unit & 1) + 2;
        unsigned fo = (unsigned)(WS_FRAG + c.c * 65536 + (unit >> 4) * 1024 + m * 64) + e2 * 2048u + ((c.lane16 & 0x30u) ^ (unsigned)((m & 4) << 3));
        if (unit >= 24) fo += c.lane16 & 0x200u;
#pragma unroll
        for (int pq = 0; pq < 2; ++pq) {
            u32x4 fr;
#pragma unroll
            for (int d = 0; d < 4; ++d) {
                const unsigned rp = rb + (d < 3 ? p8 : 0u) + (unsigned)(pq * (NF * 128) + 1024 * d);
                fr[d] = LDS(const uint16_t, rp) | ((unsigned)LDS(const uint16_t, rp + 128) << 16);
            }
            fr[3] &= dm;
            st16sc(fo + pq * 2048u, c.ws, fr);
        }
        if (unit >= 24) { st16sc(fo ^ 512u, c.ws, u32x4{0u, 0u, 0u, 0u}); st16sc((fo ^ 512u) + 2048u, c.ws, u32x4{0u, 0u, 0u, 0u}); }
    }
    SB
}
__device__ __forceinline__ void check_flags(Deep& c) {
    if (c.m3 == 0u) {
        const unsigned l4 = c.lane16 >> 2;
        const unsigned long long open = __builtin_amdgcn_ballot_w64(c.tag == c.pl) & (c.nq ? 0x7ffffff0000ull : 0x3ffffff0000ull);
        if (__builtin_expect(open != 0ull, 0)) {
#pragma unroll 1
            for (int it = 0; it < (1 << 22); ++it) {
                dma4sc(c.tad, c.ws, OFF_TAG + 256);
                asm volatile("s_waitcnt vmcnt(0)" ::: "memory");
                if ((__builtin_amdgcn_ballot_w64(LDSV(OFF_TAG + 256 + l4) == c.tag) & open) == 0ull) break;
                __builtin_amdgcn_s_sleep(2);
            }
        }
    }
    SB
}
__device__ __forceinline__ void fetch_status(Deep& c) {
    if (c.m4 == 0u) {
        if (c.nq) dma16sc(c.lane16, c.ws, OFF_NB + 1024);
        else dma4sc(c.tad, c.ws, OFF_TAG + 512);
    }
    SB
}
constexpr int x3(int P) { return (P == P_FLAG || P == P_FLAG + 1) + (P == P_POLL || P == P_POLL + 1); }
#define DTOP 2
#define DTOPL 4
#define DTOPB 4
constexpr int dtop(int P) { return P < 8 ? ((P & 3) == 2 ? DTOPB : DTOP) : DTOPL; }
template <int P, int N> __device__ __forceinline__ void dbody(Deep& c) {
    constexpr int top = dtop(P);
    m1<P - 1, N>(c);
    if constexpr (P > 10) {
        if constexpr (N < 4) rd1<P, (N ? N + 4 : 0)>(c);
        else if constexpr (N < 7) dissue<P, top + N - 4, top + N - 3>(c);
        else if constexpr (N < 15) {
            if constexpr (N == 7) { WAITCNT(dyoung(P - 1, P) + (dops(P) < top + 3 ? dops(P) : top + 3), 15); SB }
            rd1<P, (N < 11 ? N - 6 : N - 3)>(c);
        } else dissue<P, top + 3, 16>(c);
    } else {
        rd1<P, N>(c);
        if constexpr (N >= 12) dissue<P, top + N - 12, (N == 15 ? 16 : top + N - 11)>(c);
    }
    if constexpr (N < 15) dbody<P, N + 1>(c);
}
template <int P> struct DStep {
    static __device__ __forceinline__ void run(Deep& c) {
        TS(c.ts[P])
        SB
        if constexpr (P == P_FLAG) publish(c, c.tag + 1u);
        if constexpr (P == 1) { dma4((unsigned)(c.nq * 128) + ((c.lane16 >> 2) & 124u), c.wd2, OFF_WD2); SB }
        if constexpr (P == P_CHK) check_flags(c);
        if constexpr (P == P_STAT) fetch_status(c);
        dissue<P, 0, dtop(P)>(c);
        dbody<P, 0>(c);
        if constexpr (P <= 10) {
            if constexpr (P == 4) {
#pragma unroll
                for (int k = 0; k < 4; ++k) {
                    const u32x2 v = LDS(const u32x2, (unsigned)(OFF_WD2 + k * 32) + ((c.lane16 >> 5) & 24u));
                    c.w2[4 * k] = lo_f(v[0]); c.w2[4 * k + 1] = hi_f(v[0]); c.w2[4 * k + 2] = lo_f(v[1]); c.w2[4 * k + 3] = hi_f(v[1]);
                }
#pragma unroll
                for (int k = 0; k < 16; ++k) PIN(c.w2[k]);
            }
            if constexpr (P == P_POLL) { if (c.m3 == 0u) dma4sc(c.tad, c.ws, OFF_TAG + 256); }
            SB
            if constexpr (P + 1 < P_FLAG && P < 2) WAITX(dyoung(P, P + 1), 2, c.m1)
            else if constexpr (P == P_STAT) WAITX(dyoung(P, P + 1), 1, c.m4)
            else if constexpr (x3(P) > 0) WAITX(dyoung(P, P + 1), x3(P), c.m3)
            else if constexpr (P == 10) WAITCNT(63, 0);
            else WAITCNT(dyoung(P, P + 1), 0);
            if constexpr (P == P_CHK - 1) { SB c.pl = LDSV(OFF_TAG + 256 + (c.lane16 >> 2)); SB }
            BARRIER;
        }
        if constexpr (P + 1 < NP) DStep<P + 1>::run(c);
    }
};

struct Cross : Com {
    const char* tb[4]; const char* wsf;
    unsigned ga[40];
    unsigned re[2], rx[2], rx0, six, wf[2];
    s16x4 wa[2];
    bf16x8 wb[2], wr[2];
    u32x4 fe[3][2][2];
    f32x4 ew[NF], zs[4];
    u32x4 X[NP], bp[4];
    float k0, k1, k2;
    unsigned am;
    u32x4 on[4], s1, zq;
};
template <int I> __device__ __forceinline__ void issueA(Cross& c, unsigned off) { dma16o<I * 1024>(off, c.tb[I]); }
template <int K, int S, int N> __device__ __forceinline__ void sw1(Cross& c) {
    constexpr int a = 1 + 4 * K + (S == 1 ? 2 : (S == 3 ? 1 : 0)), b = 1 + 4 * K + (S == 0 ? 1 : (S == 2 ? 2 : 3)), Q = N >> 2, R = N & 3;
    float x = c.ew[2 * a + Q][R], y = c.ew[2 * b + Q][R];
    if (S >= 2) asm volatile("v_permlane32_swap_b32 %0, %1" : "+v"(x), "+v"(y)); else asm volatile("v_permlane16_swap_b32 %0, %1" : "+v"(x), "+v"(y));
    c.ew[2 * a + Q][R] = x; c.ew[2 * b + Q][R] = y;
}
template <int K, int S, int N0 = 0, int N1 = 8> __device__ __forceinline__ void swn(Cross& c) { if constexpr (N0 < N1) { sw1<K, S, N0>(c); swn<K, S, N0 + 1, N1>(c); } }
constexpr int sw0(int P) { return P <= 3 ? 0 : P <= 5 ? 8 : P == 6 ? 20 : P == 7 ? 32 : P <= 9 ? 40 : P == 10 ? 52 : 64; }
template <int P, int N0, int N1> __device__ __forceinline__ void tsw(Cross& c) {
    if constexpr (N0 < N1 && sw0(P) + N0 < sw0(P + 1)) { sw1<((sw0(P) + N0) >> 5), ((sw0(P) + N0) >> 3) & 3, (sw0(P) + N0) & 7>(c); tsw<P, N0 + 1, N1>(c); }
}
constexpr int fbuf(int P) { return P == 12 ? 2 : P & 1; }
template <int P, int N> __device__ __forceinline__ void cmm(Cross& c) {
    if constexpr (P >= 0) {
        constexpr int B = fbuf(P);
        if constexpr (N < 2)
            asm volatile("v_smfmac_f32_16x16x64_bf16 %0, %1, %2, %3" : "+v"(c.zs[P ? (P - 1) >> 2 : 3])
                         : "v"(c.on[P ? (P - 1) & 3 : 0]), "v"(__builtin_shufflevector(c.fe[B][N][0], c.fe[B][N][1], 0, 1, 2, 3, 4, 5, 6, 7)), "v"(c.six));
        else if constexpr (N < 4) c.ew[2 * P + (N & 1)] = mf0(P ? c.wb[0] : c.wr[0], c.fe[B][0][N & 1]);
        else mfa(c.ew[2 * P + (N & 1)], P ? c.wb[1] : c.wr[1], c.fe[B][1][N & 1]);
    }
    SB
}
template <int P, int N> __device__ __forceinline__ void rdE(Cross& c) {
    c.fe[fbuf(P)][N >> 1][N & 1] = LDS(const u32x4, c.re[N >> 1] + (P & 3) * SLOT + (N & 1) * 2048);
    SB
}
template <int K> __device__ __forceinline__ void zpack(Cross& c) {
    float a = c.zs[K][0], b = c.zs[K][2];
    unsigned r;
    asm volatile("v_add_f32 %1, %1, %3\n\tv_add_f32 %2, %2, %4\n\tv_cvt_pk_bf16_f32 %0, %1, %2" : "=v"(r), "+v"(a), "+v"(b) : "v"(c.zs[K][1]), "v"(c.zs[K][3]));
    c.zq[K] = r;
    SB
}
template <int K> __device__ __forceinline__ void zclear(Cross& c) { c.zs[K] = f32x4{0.f, 0.f, 0.f, 0.f}; PIN(c.zs[K]); SB }
template <int P> __device__ __forceinline__ void rdX(Cross& c) {
    constexpr int k = 1 + ((P - 1) & ~3);
    if constexpr (P == 0) c.X[0] = LDS(const u32x4, c.rx0);
    else if constexpr ((P & 3) == 2)
        asm volatile("s_mov_b32 exec_hi, 0\n\tds_read_b128 %0, %4\n\tds_read_b128 %1, %4 offset:2048\n\tds_read_b128 %2, %5\n\tds_read_b128 %3, %5 offset:2048\n\ts_mov_b32 exec_hi, -1"
                     : "=&v"(c.X[k]), "=&v"(c.X[k + 1]), "=&v"(c.X[k + 2]), "=&v"(c.X[k + 3]) : "v"(c.rx[0]), "v"(c.rx[1]) : "memory");
    else if constexpr ((P & 3) == 0)
        asm volatile("s_mov_b32 exec_lo, 0\n\tds_read_b128 %0, %4\n\tds_read_b128 %1, %4 offset:2048\n\tds_read_b128 %2, %5\n\tds_read_b128 %3, %5 offset:2048\n\ts_mov_b32 exec_lo, -1"
                     : "+v"(c.X[k]), "+v"(c.X[k + 1]), "+v"(c.X[k + 2]), "+v"(c.X[k + 3]) : "v"(c.rx[0]), "v"(c.rx[1]) : "memory");
    SB
}
template <int P> struct CStep {
    static __device__ __forceinline__ void run(Cross& c) {
        TS(c.ts[P])
        SB
        cmm<P - 1, 2>(c); rdE<P, 0>(c);
        cmm<P - 1, 3>(c); rdE<P, 1>(c);
        cmm<P - 1, 4>(c); rdE<P, 2>(c);
        cmm<P - 1, 5>(c); rdE<P, 3>(c);
        cmm<P - 1, 0>(c);
        if constexpr (P + 3 < NP) set_m0<((P + 3) & 3) * SLOT>(c.ldsb);
        cmm<P - 1, 1>(c);
        rdX<P>(c);
        constexpr bool xw = P && !(P & 1);
#define GA(J) if constexpr (P + 3 < NP) issueA<J>(c, c.ga[4 * P + (J)]); SB
        if constexpr (P == 4 || P == 8) zclear<P / 4>(c);
        if constexpr (P == 8) {
#pragma unroll
            for (int r = 0; r < 4; ++r) { c.bp[r][3] = cvt_o(c.ew[0][r], c.ew[1][r]); c.bp[r][0] = cvt_o(c.ew[2][r], c.ew[3][r]); }
            c.k0 = -2.0f * 0.7978845608028654f * 1.4426950408889634f / 64.0f; c.k1 = c.k0 * (0.044715f / 4096.0f); c.k2 = -64.0f / 1.4426950408889634f;
            c.am = (c.wf[0] & 16u) ? ~0u : 0u;
            PIN(c.k0); PIN(c.k1); PIN(c.k2); PIN(c.am);
            SB
        }
        if constexpr (P == 2 || P == 6 || P == 10) zpack<(P == 2 ? 3 : (P - 6) / 4)>(c);
        if constexpr (xw) { tsw<P, 0, 4>(c); WAITCNT(63, 0); GA(0) tsw<P, 4, 6>(c); GA(1) tsw<P, 6, 8>(c); GA(2) tsw<P, 8, 10>(c); GA(3) tsw<P, 10, 12>(c); }
        else { GA(0) GA(1) tsw<P, 0, 4>(c); GA(2) tsw<P, 4, 8>(c); GA(3) tsw<P, 8, 12>(c); }
        if constexpr (P == 3) {
            const unsigned r6 = c.lane16 * 4u + (unsigned)c.rho * 256u;
            const unsigned am = (c.lane16 >> 8) + (unsigned)c.rho;
            c.wf[0] = (r6 & 0x3c0u) | (((am << 4) ^ ((r6 >> 3) & 0x20u)) & 0x30u) | SLOT;
            c.wf[1] = c.wf[0] + OFF_B;
            const unsigned wap = OFF_SW2 + (c.lane16 & 0x3c0u) + ((c.lane16 >> 1) & 0x18u);
            c.wa[0] = tr_read(wap); c.wa[1] = tr_read(wap + 32);
            const unsigned r2 = (c.lane16 >> 3) & 30u, s1 = OFF_SW1 + min(r2, 24u), sg = s1 + (am & 3u) * 52u;
#pragma unroll
            for (int k = 0; k < 4; ++k) {
                const unsigned a = (k < 3 ? sg : s1) + k * 208;
                const unsigned v = LDS(const uint16_t, a) | ((unsigned)LDS(const uint16_t, a + 26) << 16);
                c.s1[k] = r2 < 26u ? v : 0u;
            }
            PIN(c.s1);
            PIN(c.wf[0]); PIN(c.wf[1]); PIN(c.wa[0]); PIN(c.wa[1]);
        }
        SB
        WAITCNT(P < 10 ? 8 : 0, 0); BARRIER;
        if constexpr (P < 10) CStep<P + 1>::run(c);
    }
};

#define TE0 1
#define NB1 1
#define TN 6
#define TOUCH asm volatile("s_mov_b32 m0, %2\n\ts_nop 0\n\tbuffer_load_dword %0, %1, 0 offen lds" :: "v"(min(lane16, (TN - 1) * 16u) * 8u + (unsigned)((bid * 4 + cw) * (TN * 128))), "s"(d.rw1), "n"(OFF_JNK) : "memory");
#define WPRE 6
__global__ __launch_bounds__(512) void k_main(const int* __restrict__ idx, const uint16_t* __restrict__ table,
        const uint16_t* __restrict__ w_bi, const uint16_t* __restrict__ w_deep1, const uint16_t* __restrict__ w_cross,
        unsigned* __restrict__ ws32, const uint16_t* __restrict__ se_w1, const uint16_t* __restrict__ se_w2,
        const uint16_t* __restrict__ w_deep2_, __hip_bfloat16* __restrict__ out_, unsigned long long* __restrict__ stamps) {
    SB
    const int tid = threadIdx.x, lane = tid & 63;
    const int w = __builtin_amdgcn_readfirstlane(tid >> 6);
    const int bid = blockIdx.x;
    const int xcd = bid & 7, slot = bid >> 3;
    const int st = xcd * 8 + (slot >> 2), nq = slot & 3;
    const int s0 = st * 64;
    const int rho = (st + nq) & 3, cw = w & 3;
    const int l15 = lane & 15, g = lane >> 4;
    SB const unsigned long long zpa = __builtin_amdgcn_s_memrealtime(); SB
    LDS(u32x2, OFF_FL + tid * 8) = u32x2{0u, 0u};
    unsigned long long pc;
    {
        const unsigned ln = min((unsigned)(4 + w * 4 + (lane & 3) + ((lane >> 2) & 1) * (32 + nq * 32)), (unsigned)PFC) * 128u;
        asm volatile("s_getpc_b64 %0\n\ts_mov_b32 m0, %2\n\ts_mov_b64 exec, 0xff\n\tglobal_load_lds_dword %1, %0\n\ts_mov_b64 exec, -1"
                     : "=&s"(pc) : "v"(ln), "n"(OFF_JNK) : "memory");
    }
    SB
    float gc0 = -2.0f * 0.7978845608028654f * 1.4426950408889634f, gc1 = gc0 * 0.044715f;
#define FIN_ST(c)
#define FIN_SA(c)
#define FINISH(c) if ((1) && (1)) { const unsigned long long tb_ = __builtin_amdgcn_s_memrealtime(), tn = __builtin_amdgcn_s_memrealtime(), te = tn + 8 * (tb_ - zpa); while (__builtin_amdgcn_s_memrealtime() < te) __builtin_amdgcn_s_sleep(2); } STAMP(5) FIN_ST(c) FIN_SA(c) __builtin_amdgcn_endpgm(); __builtin_unreachable();
    unsigned lane16 = (unsigned)lane * 16u;
    int deep = w >> 2;
    Deep d;
    d.ldsb = (unsigned)(cw * 4096);
    unsigned o2 = min(lane16, 624u), im0 = (unsigned)(OFF_IDX + cw * 2048), io = (unsigned)((s0 + 16 * cw) * (NF * 4));
    mkboff(d, lane, cw, nq);
#pragma unroll
    for (int k = 0; k < 4; ++k) { d.sG[k] = (unsigned)(((k + rho) & 3) * (2 * 32768)); PINS(d.sG[k]); }
    d.unit = xcd + 8 * ((st & 7) >> 1);
    d.m1 = (unsigned)((nq ^ 1) | (st & 1) | ((d.unit + 32 - NF) >> 5));
    PIN(lane16); PIN(o2); PINS(im0); PINS(io); PINS(deep); PINS(d.ldsb); PINS(d.m1);
    asm volatile("s_mov_b32 m0, %0" :: "s"(im0) : "memory");
    SB
    asm volatile("" :: "s"(idx), "s"(table), "s"(se_w1), "s"(se_w2), "s"(w_bi), "s"(w_deep1), "s"(w_cross), "s"(ws32));
    asm volatile("s_cmp_lg_u32 %3, 0\n\ts_cbranch_scc1 1f\n\tglobal_load_lds_dwordx4 %0, %2\n\tglobal_load_lds_dwordx4 %1, %2 offset:1024\n1:"
                 :: "v"(lane16), "v"(o2), "s"((const char*)idx + io), "s"(deep) : "memory", "scc");
    SB
    typedef const __attribute__((address_space(4))) unsigned long long* kap;
    kap ka = (kap)__builtin_amdgcn_kernarg_segment_ptr();
    PINS(ka);
    d.wd2 = (const char*)ka[8];
    const gp out = (gp)ka[9];
    unsigned long long w1p;
    asm volatile("s_mov_b64 %0, %1" : "=s"(w1p) : "s"(w_deep1));
    d.rw1 = __builtin_amdgcn_make_buffer_rsrc((void*)w1p, (short)0, NF * 32768, 0x00020000);
    if (TE0 && deep) { TOUCH }
    prepB<0>(d);
    TS(tz1)
    if (!NB1) BARRIER;
    TS(tz2)
    Cross x;
    x.c = cw; x.nq = nq; x.rho = rho; x.st = st; x.lane16 = lane16; x.ws = (char*)ws32; x.ldsb = d.ldsb;
    unsigned srcA, srcB, ib[4];
    u32x2 ia[NP], ic[NP];
    if (deep) {
        issueB<0>(d); issueB<1>(d); issueB<2>(d); issueB<3>(d);
        SB
        dma16((unsigned)((cw & 1) * 4096 + (lane >> 1) * 128 + nq * 32 + (lane & 1) * 16), w_bi, OFF_WBI + (cw & 1) * 1024);
        dma4((unsigned)min(cw * 64 + lane, 168) * 4u, se_w1, OFF_SW1 + cw * 256);
        const int k = (l15 & 1) | ((l15 >> 2) & 2), r = 4 * cw + g;
        dma4((unsigned)((r < 13 ? r : 12) * (NF * 2) + k * 16 + (k == 3 ? 0 : ((l15 + 2 * rho) & 6) * 2)), se_w2, OFF_SW2 + cw * 256);
        SB
        if (__builtin_expect(d.m1 == 0u, 1)) {
            const unsigned r0 = (unsigned)(16 * cw + (lane >> 3)), lo = (lane16 & 0x70u) + (unsigned)(d.unit * 128);
            dma16(min(r0, 2u * NF - 1) * (NF * 128) + lo, w_cross, OFF_RAW + cw * 2048);
            dma16(min(r0 + 8u, 2u * NF - 1) * (NF * 128) + lo, w_cross, OFF_RAW + cw * 2048 + 1024);
            dma4sc((lane16 >> 2) + (unsigned)(cw * 256), ws32, OFF_NB + cw * 256);
        }
        if (cw == 3) dma4sc(tag_addr(lane, d.unit, nq, st, xcd), ws32, OFF_TAG);
        if ((cw ^ 2) + nq == 0 && lane < 32) st16sc(lane16, (char*)ws32 + WS_SUM + st * 512, u32x4{0u, 0u, 0u, 0u});
        SB
        if (!TE0) { TOUCH }
        if (nq != 3) { prepB<1>(d); asm volatile("s_nop 0"); issueB<0>(d); issueB<1>(d); issueB<2>(d); issueB<3>(d); }
        SB
    } else {
        Cross& c = x;
        srcA = (unsigned)(((lane & 7) ^ g) * 16); srcB = srcA ^ 64u;
        unsigned ib0 = (unsigned)(OFF_IDX + cw * 2048 + (lane >> 3) * 104);
#pragma unroll
        for (int k = 0; k < 4; ++k) { ib[k] = ib0 + (unsigned)(((k + rho) & 3) * 8); PIN(ib[k]); c.tb[k] = (const char*)table - k * 1024; PINS(c.tb[k]); }
        PIN(srcA); PIN(srcB); PIN(ib0);
        set_m0<0>(c.ldsb);
        SB
        WAITCNT(0, 15);
        asm volatile("" ::: "memory");
        STAMP(1)
        TP(c.tq[0])
        ia[0] = LDS(const u32x2, ib0 + 96); ic[0] = LDS(const u32x2, ib0 + 96 + 832);
#pragma unroll
        for (int q = 1; q < 3; ++q) { ia[q] = LDS(const u32x2, ib[q - 1]); ic[q] = LDS(const u32x2, ib[q - 1] + 832); }
        SB
#define GATHER(q) issueA<0>(c, ia[q][0] * 128u + srcA); issueA<1>(c, ic[q][0] * 128u + srcB); issueA<2>(c, ia[q][1] * 128u + srcA); issueA<3>(c, ic[q][1] * 128u + srcB);
        GATHER(0)
        set_m0<SLOT>(c.ldsb);
        SB
        GATHER(1)
        set_m0<2 * SLOT>(c.ldsb);
        TP(c.tq[1])
    }
    TS(tz3)
    int crs = deep ^ 1;
    PINS(crs); PINS(deep);
    if (crs == 0) {
        Deep& c = d;
        int lane;
        asm volatile("v_mbcnt_lo_u32_b32 %0, -1, 0\n\tv_mbcnt_hi_u32_b32 %0, -1, %0" : "=v"(lane));
        const int l15 = lane & 15, g = lane >> 4, tid = w * 64 + lane;
        c.c = cw; c.nq = nq; c.rho = rho; c.st = st; c.lane16 = (unsigned)lane * 16u; c.ws = (char*)ws32;
        c.m3 = (unsigned)(cw ^ 3); c.m4 = c.m3 | min(c.m1, (unsigned)nq);
        c.tad = tag_addr(lane, c.unit, nq, st, xcd);
        PIN(c.tad);
        SB
        mkboff(d, lane, cw, nq);
        {
            const unsigned a0 = (unsigned)(cw * 4096 + (cw & 1) * 2048 + l15 * 128 + ((4 * (cw >> 1) + g) ^ (l15 >> 1)) * 16);
            const int q = l15 >> 2, p = l15 & 3;
            const unsigned b0 = (unsigned)(OFF_B + cw * 4096 + (8 * g + q) * 128 + ((p >> 1) ^ (q & 2) ^ ((g & 1) << 2)) * 16 + (p & 1) * 8);
#pragma unroll
            for (int k = 0; k < 4; ++k) { c.ra[k] = a0 ^ (unsigned)(k * 4096); c.rb[k] = b0 ^ (unsigned)(k * 32); PIN(c.ra[k]); PIN(c.rb[k]); }
#pragma unroll
            for (int k = 0; k < 16; ++k) { c.ac[k >> 2][k & 3] = f32x4{0.f, 0.f, 0.f, 0.f}; PIN(c.ac[k >> 2][k & 3]); }
        }
        unsigned exw[4], fl = (c.lane16 >> 2) + (unsigned)(OFF_FL + cw * 256), fr = c.lane16 + OFF_FL, s2 = c.lane16 >> 3, s8 = c.lane16 >> 1;
#pragma unroll
        for (int k = 0; k < 4; ++k) { exw[k] = c.lane16 + (unsigned)(OFF_B + (cw ^ k) * 4096); PIN(exw[k]); }
        double big = 0x1.8p52;
        float va[2] = {0.f, 0.f};
        const unsigned dl = (unsigned)(l15 - 4 * g), on = (dl & 1u) ? 0x3f800000u : 0x3f80u;
        u32x4 unit = {dl < 2u ? on : 0u, dl - 2u < 2u ? on : 0u, dl < 2u ? on : 0u, dl - 2u < 2u ? on : 0u};
        PIN(fl); PIN(fr); PIN(s2); PIN(s8); PIN(big); PIN(gc0); PIN(gc1); PIN(va[0]); PIN(va[1]); PIN(unit);
        c.wsf = c.ws + WS_FRAG + nq * 65536 + cw * 4096;
        c.pmask = cw != 3 ? 0ull : (c.m1 == 0u ? 0x1ffull : 0x100ull);
        PINS(c.pmask); PINS(c.m3); PINS(c.m4); PINS(c.wsf);
        if (nq == 3) { prepB<1>(c); asm volatile("s_nop 0"); issueB<0>(c); issueB<1>(c); issueB<2>(c); issueB<3>(c); }
        TP(c.tq[0])
        WAITCNT(4, 0); BARRIER;
        TP(c.tq[1])
        prepB<2>(c); asm volatile("s_nop 0"); issueB<0>(c); issueB<1>(c); issueB<2>(c); issueB<3>(c);
        TP(c.tq[2])
        produce(c);
        c.tag = LDSV(OFF_TAG + (c.lane16 >> 2));
        PIN(c.tag);
        TP(c.tq[3])
        BARRIER;
        TP(c.tq[4])
        gp sums = (gp)c.ws + (unsigned)(WS_SUM + (s0 + 16 * cw) * 8), outp = out + (unsigned)((s0 + 16 * cw) * 2);
        PINS(sums); PINS(outp);
        DStep<0>::run(c);
        TS(c.ts[13])
        u32x4 px[3][2];
#define EXC(e) { _Pragma("unroll") for (int k = 0; k < 8; ++k) px[(e) - 1][k >> 2][k & 3] = cvt_o(c.ac[e][k >> 1][2 * (k & 1)], c.ac[e][k >> 1][2 * (k & 1) + 1]); SB }
#define EXA(e, k) (((e) == 3 ? 3 * SLOT : ((e) - 1) * 2048) + (k) * 1024)
#define EXW(e) { LDS(u32x4, exw[0] + EXA(e, 0)) = px[(e) - 1][0]; LDS(u32x4, exw[0] + EXA(e, 1)) = px[(e) - 1][1]; SB }
#define EX2(e, k) { px[(e) - 1][(k) >> 1][2 * ((k) & 1)] = cvt_o(c.ac[e][k][0], c.ac[e][k][1]); px[(e) - 1][(k) >> 1][2 * ((k) & 1) + 1] = cvt_o(c.ac[e][k][2], c.ac[e][k][3]); SB }
        WAITCNT(0, 15); SB
        LDS(unsigned, fl + 2048) = fl; SB
        TE(c.tq[0])
        m4<12, 3>(c); m4<12, 2>(c);
        mk<12, 1, 0>(c); mk<12, 1, 1>(c); EX2(3, 0) mk<12, 1, 2>(c); EX2(3, 1) mk<12, 1, 3>(c); EX2(3, 2)
        mk<12, 0, 0>(c); EX2(3, 3) mk<12, 0, 1>(c); EX2(2, 0) mk<12, 0, 2>(c); EX2(2, 1) mk<12, 0, 3>(c); EX2(2, 2)
        EX2(2, 3) EXC(1)
        EXW(3) EXW(2) EXW(1)
        TE(c.tq[1])
        SB
        LDS(unsigned, fl + 1024) = fl;
        SB
        unsigned long long watch = 0ull;
        if (cw == 3) {
            watch = __builtin_amdgcn_ballot_w64(LDSV(OFF_TAG + 512 + (c.lane16 >> 2)) == c.tag) & (nq ? 0ull : 0xe00ull);
            if (c.m1 == 0u) {
                const u32x4 e4 = LDS(const u32x4, OFF_NB + c.lane16), l4 = LDS(const u32x4, OFF_NB + 1024 + c.lane16);
                watch |= __builtin_amdgcn_ballot_w64(e4[0] == l4[0] || e4[1] == l4[1] || e4[2] == l4[2] || e4[3] == l4[3]);
            }
            publish(c, c.tag + 2u);
        }
        TE(c.tq[2])
#pragma unroll 1
        for (int it = 0; it < (1 << 20) && __builtin_amdgcn_ballot_w64(LDSV(fr + 1024) == 0u) != 0ull; ++it) { }
        asm volatile("" ::: "memory");
        TE(c.tq[3])
        u32x2 pa[4], pb[4];
#pragma unroll
        for (int k = 0; k < 2; ++k) px[2][k] = LDS(const u32x4, exw[3] + EXA(3, k));
#pragma unroll
        for (int k = 0; k < 4; ++k) {
            pa[k] = *(volatile const __attribute__((address_space(3))) u32x2*)(size_t)(exw[1] + EXA(1, k >> 1) + (k & 1) * 8);
            pb[k] = *(volatile const __attribute__((address_space(3))) u32x2*)(size_t)(exw[2] + EXA(2, k >> 1) + (k & 1) * 8);
        }
        SB
#pragma unroll
        for (int k = 0; k < 4; ++k)
            asm volatile("v_mfma_f32_16x16x16_bf16 %0, %1, %2, %0" : "+v"(c.ac[0][k]) : "v"(u32x2{unit[0], unit[1]}), "v"(u32x2{px[2][k >> 1][2 * (k & 1)], px[2][k >> 1][2 * (k & 1) + 1]}));
        asm volatile("s_nop 2");
#pragma unroll
        for (int k = 0; k < 4; ++k) mfa(c.ac[0][k], unit, u32x4{pa[k][0], pa[k][1], pb[k][0], pb[k][1]});
        asm volatile("s_nop 4");
#pragma unroll
        for (int k = 0; k < 4; ++k) {
            float x[4] = {c.ac[0][k][0], c.ac[0][k][1], c.ac[0][k][2], c.ac[0][k][3]}, y[4];
            asm volatile("v_mul_f32 %4, %0, %0\n\tv_mul_f32 %5, %1, %1\n\tv_mul_f32 %6, %2, %2\n\tv_mul_f32 %7, %3, %3\n\t"
                         "v_fma_f32 %4, %4, %15, %14\n\tv_fma_f32 %5, %5, %15, %14\n\tv_fma_f32 %6, %6, %15, %14\n\tv_fma_f32 %7, %7, %15, %14\n\t"
                         "v_mul_f32 %4, %4, %0\n\tv_mul_f32 %5, %5, %1\n\tv_mul_f32 %6, %6, %2\n\tv_mul_f32 %7, %7, %3\n\t"
                         "v_exp_f32 %4, %4\n\tv_exp_f32 %5, %5\n\tv_exp_f32 %6, %6\n\tv_exp_f32 %7, %7\n\t"
                         "v_mul_f32 %0, %0, %10\n\tv_mul_f32 %1, %1, %11\n\tv_mul_f32 %2, %2, %12\n\tv_mul_f32 %3, %3, %13\n\t"
                         "v_add_f32 %4, 1.0, %4\n\tv_add_f32 %5, 1.0, %5\n\tv_add_f32 %6, 1.0, %6\n\tv_add_f32 %7, 1.0, %7\n\t"
                         "v_rcp_f32 %4, %4\n\tv_rcp_f32 %5, %5\n\tv_rcp_f32 %6, %6\n\tv_rcp_f32 %7, %7\n\t"
                         "v_fmac_f32 %8, %0, %4\n\tv_fmac_f32 %9, %1, %5\n\tv_fmac_f32 %8, %2, %6\n\tv_fmac_f32 %9, %3, %7"
                         : "+v"(x[0]), "+v"(x[1]), "+v"(x[2]), "+v"(x[3]), "=&v"(y[0]), "=&v"(y[1]), "=&v"(y[2]), "=&v"(y[3]), "+v"(va[0]), "+v"(va[1])
                         : "v"(c.w2[4 * k]), "v"(c.w2[4 * k + 1]), "v"(c.w2[4 * k + 2]), "v"(c.w2[4 * k + 3]), "v"(gc0), "v"(gc1));
        }
        TE(c.tq[4])
        const float v = sum16(sum32(va[0] + va[1]));
        TS(c.ts[14])
        if (__builtin_amdgcn_inverse_ballot_w64(0xffffull)) deliver(c, v, 2 * nq, s2, s8, big, sums, outp);
        if (__builtin_expect(watch != 0ull, 0)) {
            const u32x4 e4 = LDS(const u32x4, OFF_NB + c.lane16), l4 = LDS(const u32x4, OFF_NB + 1024 + c.lane16);
            bool opn[4];
#pragma unroll
            for (int k = 0; k < 4; ++k) opn[k] = c.m1 == 0u && e4[k] == l4[k];
            bool own = nq == 0 && lane >= 9 && lane < 12 && LDSV(OFF_TAG + 512 + lane * 4) == c.tag;
            unsigned f = c.tag + 3u;
#pragma unroll 1
            for (int it = 0; it < (1 << 22) && __builtin_amdgcn_ballot_w64(opn[0] || opn[1] || opn[2] || opn[3] || own) != 0ull; ++it) {
                bool chg = false;
#pragma unroll
                for (int k = 0; k < 4; ++k)
                    if (opn[k] && __hip_atomic_load(ws32 + lane * 4 + k, __ATOMIC_RELAXED, __HIP_MEMORY_SCOPE_AGENT) != e4[k]) { opn[k] = false; chg = true; }
                if (own && __hip_atomic_load(ws32 + (c.tad >> 2), __ATOMIC_RELAXED, __HIP_MEMORY_SCOPE_AGENT) != c.tag) { own = false; chg = true; }
                if (__builtin_amdgcn_ballot_w64(chg) != 0ull) { publish(c, f); ++f; }
                else __builtin_amdgcn_s_sleep(2);
            }
        }
        FINISH(c)
    } else {
        Cross& c = x;
#pragma unroll
        for (int q = 3; q < NP; ++q) { ia[q] = LDS(const u32x2, ib[(q - 1) & 3] + ((q - 1) >> 2) * 32); ic[q] = LDS(const u32x2, ib[(q - 1) & 3] + ((q - 1) >> 2) * 32 + 832); }
#pragma unroll
        for (int q = 3; q < NP; ++q) {
            c.ga[4 * q - 12] = ia[q][0] * 128u + srcA; c.ga[4 * q - 11] = ic[q][0] * 128u + srcB;
            c.ga[4 * q - 10] = ia[q][1] * 128u + srcA; c.ga[4 * q - 9] = ic[q][1] * 128u + srcB;
        }
#pragma unroll
        for (int k = 0; k < 40; ++k) PIN(c.ga[k]);
        {
            const int G = (g + rho) & 3;
            const unsigned ea = (unsigned)(cw * 4096 + l15 * 128);
            c.re[0] = ea + (unsigned)((g ^ (l15 >> 1)) * 16); c.re[1] = c.re[0] ^ 64u;
            c.rx[0] = ea + (unsigned)(((g + 1) & 3) * SLOT + ((2 * nq) ^ (l15 >> 1)) * 16); c.rx[1] = c.rx[0] ^ 16u;
            c.rx0 = ea + (unsigned)((G & 1) * 2048 + ((2 * nq + (G >> 1)) ^ (l15 >> 1)) * 16);
            c.six = (l15 & 1) ? 0xeeeeu : 0x4444u;
#pragma unroll
            for (int k = 0; k < 4; ++k) { const unsigned o = (l15 >> 1) == 2 * k + (g >> 1) ? 0x3f803f80u : 0u; c.on[k] = u32x4{o, o, o, o}; PIN(c.on[k]); }
            PIN(c.re[0]); PIN(c.re[1]); PIN(c.rx[0]); PIN(c.rx[1]); PIN(c.rx0); PIN(c.six);
            zclear<3>(c); zclear<0>(c);
        }
        SB
        BARRIER;
        GATHER(2)
        TP(c.tq[2])
        {
            const int q = (lane >> 2) & 3, p = lane & 3;
            const unsigned a0 = (unsigned)(OFF_WBI + (8 * g + q) * 32 + p * 8), a1 = (unsigned)(OFF_WBI + (8 * g + q) * 32 + ((p + rho) & 3) * 8);
#pragma unroll
            for (int k = 0; k < 2; ++k) {
                c.wb[k] = CAT8(tr_read(a0 + k * 1024), tr_read(a0 + k * 1024 + 128));
                c.wr[k] = CAT8(tr_read(a1 + k * 1024), tr_read(a1 + k * 1024 + 128));
            }
#pragma unroll
            for (int k = 0; k < 2; ++k) { PIN(c.wb[k]); PIN(c.wr[k]); }
        }
        gp sums = (gp)c.ws + (unsigned)(WS_SUM + (s0 + 16 * cw) * 8), outp = out + (unsigned)((s0 + 16 * cw) * 2);
        PINS(sums); PINS(outp);
        c.wsf = c.ws + WS_FRAG + 32768 + nq * 65536 + cw * 4096; PINS(c.wsf);
        TP(c.tq[3])
        WAITCNT(8, 0); BARRIER;
        TP(c.tq[4])
        __builtin_amdgcn_s_setprio(1);
        CStep<0>::run(c);
        TS(c.ts[11])
        u32x4 bq[4];
        u32x4 (&bp)[4] = c.bp;
        f32x2 T[6][8];
#define SPLIT(s0, s1, i0, i1) { _Pragma("unroll") for (int s = s0; s < s1; ++s) _Pragma("unroll") for (int i_ = i0; i_ < i1; ++i_) { \
            const f32x4 x = c.ew[2 * (1 + 4 * (s >> 1) + (i_ >> 1)) + (s & 1)]; T[s][i_] = f32x2{x[2 * (i_ & 1)], x[2 * (i_ & 1) + 1]}; } }
#define CB(B, i, h, k) { B[(2 * (i) + (h)) & 3][k] = cvt_o(T[2 * (k)][i][h], T[2 * (k) + 1][i][h]); }
#define SC(i, k) { asm volatile("v_pk_mul_f32 %0, %1, %0 op_sel_hi:[0,1]" : "+v"(T[2 * (k)][i]) : "v"(ao[k])); asm volatile("v_pk_mul_f32 %0, %1, %0 op_sel:[1,0]" : "+v"(T[2 * (k) + 1][i]) : "v"(ao[k])); }
        SPLIT(0, 4, 0, 8)
        SB
#define FPOS 2
#define FI(n) if ((n) >= FPOS && (n) < FPOS + 4) { if (!(((n) - FPOS) & 1)) { set_m0<FOFF[2 + ((((n) - FPOS) >> 1) & 1)]>(c.ldsb); asm volatile("s_nop 0"); } \
            dma16sco<(((n) - FPOS) & 1) * 2048>(c.lane16, c.wsf + (((n) - FPOS) >> 1) * 16384); dma16sco<(((n) - FPOS) & 1) * 2048 + 1024>(c.lane16, c.wsf + (((n) - FPOS) >> 1) * 16384); SB }
        cmm<10, 2>(c); rdE<11, 0>(c);
        cmm<10, 3>(c); rdE<11, 1>(c);
        cmm<10, 4>(c); rdE<11, 2>(c);
        cmm<10, 5>(c); rdE<11, 3>(c);
        cmm<10, 0>(c); rdE<12, 0>(c); rdE<12, 1>(c);
        cmm<10, 1>(c); rdE<12, 2>(c); rdE<12, 3>(c);
        FI(0)
        CB(bp, 0, 0, 1) CB(bp, 0, 1, 1) CB(bp, 1, 0, 1) CB(bp, 1, 1, 1)
        cmm<11, 0>(c); sw1<2, 0, 0>(c);
        cmm<11, 1>(c); sw1<2, 0, 1>(c);
        cmm<12, 0>(c); sw1<2, 0, 2>(c);
        cmm<12, 1>(c); sw1<2, 0, 3>(c);
        STAMP(2)
        TS(c.ts[12])
        cmm<11, 2>(c); sw1<2, 0, 4>(c);
        cmm<11, 3>(c); sw1<2, 0, 5>(c);
        cmm<11, 4>(c); sw1<2, 0, 6>(c);
        cmm<11, 5>(c); zpack<2>(c);
        cmm<12, 2>(c); sw1<2, 0, 7>(c);
        const f32x4 hp = mf0(c.s1, c.zq);
        cmm<12, 3>(c); cmm<12, 4>(c); cmm<12, 5>(c);
        rdX<12>(c);
        FI(1)
        float hq[4];
        asm volatile("v_mul_f32 %0, %4, %4\n\tv_mul_f32 %1, %5, %5\n\tv_mul_f32 %2, %6, %6\n\tv_mul_f32 %3, %7, %7\n\t"
                     "v_fma_f32 %0, %0, %9, %8\n\tv_fma_f32 %1, %1, %9, %8\n\tv_fma_f32 %2, %2, %9, %8\n\tv_fma_f32 %3, %3, %9, %8\n\t"
                     "v_mul_f32 %0, %0, %4\n\tv_mul_f32 %1, %1, %5\n\tv_mul_f32 %2, %2, %6\n\tv_mul_f32 %3, %3, %7\n\t"
                     "v_exp_f32 %0, %0\n\tv_exp_f32 %1, %1\n\tv_exp_f32 %2, %2\n\tv_exp_f32 %3, %3\n\t"
                     "v_fma_f32 %0, %0, %10, %10\n\tv_fma_f32 %1, %1, %10, %10\n\tv_fma_f32 %2, %2, %10, %10\n\tv_fma_f32 %3, %3, %10, %10\n\t"
                     "v_rcp_f32 %0, %0\n\tv_rcp_f32 %1, %1\n\tv_rcp_f32 %2, %2\n\tv_rcp_f32 %3, %3\n\t"
                     "v_mul_f32 %0, %0, %4\n\tv_mul_f32 %1, %1, %5\n\tv_mul_f32 %2, %2, %6\n\tv_mul_f32 %3, %3, %7\n\t"
                     "v_cvt_pk_bf16_f32 %0, %0, %1\n\tv_cvt_pk_bf16_f32 %1, %2, %3"
                     : "=&v"(hq[0]), "=&v"(hq[1]), "=&v"(hq[2]), "=&v"(hq[3]) : "v"(hp[0]), "v"(hp[1]), "v"(hp[2]), "v"(hp[3]), "v"(c.k0), "v"(c.k1), "v"(c.k2));
        FI(2)
        const f32x2 hb = {hq[0], hq[1]};
        swn<2, 1, 0, 2>(c);
        f32x4 av[2];
        asm volatile("v_mfma_f32_16x16x16_bf16 %0, %1, %2, 0" : "=&v"(av[0]) : "v"(c.wa[0]), "v"(hb));
        asm volatile("v_mfma_f32_16x16x16_bf16 %0, %1, %2, 0" : "=&v"(av[1]) : "v"(c.wa[1]), "v"(hb));
        FI(3)
        swn<2, 1, 2, 8>(c);
        swn<2, 2, 0, 2>(c);
        FI(4)
        TS(c.ts[13])
        SB
        float as[8];
#pragma unroll
        for (int r = 0; r < 8; ++r) as[r] = exp_o(av[r >> 2][r & 3]);
        asm volatile("v_add_f32 %0, 1.0, %0\n\tv_add_f32 %1, 1.0, %1\n\tv_add_f32 %2, 1.0, %2\n\tv_add_f32 %3, 1.0, %3\n\tv_add_f32 %4, 1.0, %4\n\tv_add_f32 %5, 1.0, %5\n\tv_add_f32 %6, 1.0, %6\n\tv_add_f32 %7, 1.0, %7\n\t"
                     "v_rcp_f32 %0, %0\n\tv_rcp_f32 %1, %1\n\tv_rcp_f32 %2, %2\n\tv_rcp_f32 %3, %3\n\tv_rcp_f32 %4, %4\n\tv_rcp_f32 %5, %5\n\tv_rcp_f32 %6, %6\n\tv_rcp_f32 %7, %7"
                     : "+v"(as[0]), "+v"(as[1]), "+v"(as[2]), "+v"(as[3]), "+v"(as[4]), "+v"(as[5]), "+v"(as[6]), "+v"(as[7]));
        FI(5)
        unsigned flg = LDSV(c.wf[1] + (OFF_FL + 2048 - OFF_B - SLOT));
        SB
        swn<2, 2, 2, 8>(c);
        FI(6)
        SPLIT(4, 6, 0, 2) SPLIT(4, 6, 4, 6)
        TE(c.tq[0])
        SB
        WAITCNT(63, 0);
        wait_set(flg, c.wf[1] + (OFF_FL + 2048 - OFF_B - SLOT));
        SB
#define WSEQ(n) LDS(const u32x4, c.wf[(((n) >> 4) & 1) ^ 1] + ((n) >> 5) * SLOT + ((n) & 15) * 1024)
        u32x4 wq[64];
#pragma unroll
        for (int r = 0; r < WPRE; ++r) wq[r] = WSEQ(r);
        SB
        CB(bp, 0, 0, 2) CB(bp, 0, 1, 2)
        f32x2 ao[4] = {{as[0], as[1]}, {as[2], as[3]}, {as[4], as[5]}, {as[6], as[7]}};
        {
            f32x2 m[4] = {{c.ew[0][0], c.ew[0][1]}, {c.ew[1][0], c.ew[1][1]}, {c.ew[0][2], c.ew[0][3]}, {c.ew[1][2], c.ew[1][3]}};
#pragma unroll
            for (int r = 0; r < 4; r += 2) {
                asm volatile("v_pk_mul_f32 %0, %1, %0 op_sel_hi:[0,1]" : "+v"(m[r]) : "v"(ao[3]));
                asm volatile("v_pk_mul_f32 %0, %1, %0 op_sel:[1,0]" : "+v"(m[r + 1]) : "v"(ao[3]));
            }
            SC(0, 0) SC(0, 1) SC(0, 2)
#pragma unroll
            for (int r = 0; r < 4; ++r) bq[r][3] = cvt_o(m[r & 2][r & 1], m[(r & 2) + 1][r & 1]);
        }
        ao[3][0] = sel(c.am, as[7], as[6]);
        PIN(ao[3]);
        SB
        TE(c.tq[1])
        f32x4 dg;
        f32x4 u[2][4];
        f32x2 sv[2][4];
        u32x4 pk[7];
#define FM(h, tl) { _Pragma("unroll") for (int q = 0; q < 2; ++q) asm volatile("v_pk_fma_f32 %0, %1, %2, %3" : "=v"(sv[h][2 * (tl) + q]) : "v"(f32x2{u[h][2 + (tl)][2 * q], u[h][2 + (tl)][2 * q + 1]}), "v"(ao[2 * (tl) + q]), "v"(f32x2{u[h][tl][2 * q], u[h][tl][2 * q + 1]})); }
#define PK(i, s) { pk[s][(i) & 3] = cvt_o(sv[0][(s) >> 1][(s) & 1], sv[1][(s) >> 1][(s) & 1]); }
#define DOT(eh, s) { const u32x4 x_ = (s) < 6 ? c.X[1 + 4 * ((s) >> 1) + ((s) & 1) + 2 * (eh)] : c.X[0]; if ((eh) || (s)) mfa(dg, pk[s], x_); else dg = mf0(pk[s], x_); }
#pragma unroll
        for (int i = 0; i < 8; ++i) {
            if (i == 3) { SB wait_set(flg, c.wf[1] + (OFF_FL - OFF_B - SLOT)); SB }
            if (i == 1) { SB WAITCNT(0, 15); LDS(unsigned, (c.lane16 >> 2) + (unsigned)(OFF_FL + cw * 256)) = c.wf[0]; SB }
            if (i == 2) { TE(c.tq[2]) }
            if (i == 4) { TE(c.tq[3]) }
            if (i == 1) { SPLIT(4, 6, 2, 4) SPLIT(4, 6, 6, 8) }
#pragma unroll
            for (int j = 0; j < 8; ++j) {
                const int n = 8 * i + j, h = j >> 2;
                const bool nx = i < 7, pv = i >= 1;
                SB
                if (!(j & 1)) { if (n + WPRE <= 64) WAITCNT(63, WPRE - 2); SB }
                u[h][j & 3] = mf0(wq[n], (j & 2) ? bq[(2 * i + h) & 3] : bp[(2 * i + h) & 3]);
                SB
                if (n == 17) flg = LDSV(c.wf[1] + (OFF_FL - OFF_B - SLOT));
                if (n + WPRE < 64) wq[n + WPRE] = WSEQ(n + WPRE);
                SB
                if (i == 0) { if (j == 2) swn<2, 3, 0, 1>(c); if (j == 3) swn<2, 3, 1, 2>(c); if (j == 4) swn<2, 3, 2, 4>(c); if (j == 5) swn<2, 3, 4, 5>(c); if (j == 6) swn<2, 3, 5, 7>(c); if (j == 7) swn<2, 3, 7, 8>(c); }
                if (j == 0) { CB(bq, i, 0, 0) if (i == 5) DOT(0, 4) CB(bq, i, 0, 1) }
                if (j == 1) { CB(bq, i, 0, 2) if (i == 5) DOT(0, 5) CB(bq, i, 1, 0) CB(bq, i, 1, 1) }
                if (j == 2) { CB(bq, i, 1, 2) if (i == 5) DOT(0, 6) if (pv) PK(i - 1, 0) }
                if (j == 3) { if (pv) { PK(i - 1, 1) if (i == 4) DOT(0, 0) PK(i - 1, 2) } }
                if (j == 4) { if (pv) { PK(i - 1, 3) if (i == 4) DOT(0, 1) PK(i - 1, 4) } }
                if (j == 5) { if (pv) { PK(i - 1, 5) if (i == 4) DOT(0, 2) PK(i - 1, 6) } }
                if (j == 6) { if (nx && i) { CB(bp, i + 1, 0, 0) if (i == 4) DOT(0, 3) CB(bp, i + 1, 1, 0) } }
                if (j == 7) {
                    if (nx && i) { CB(bp, i + 1, 0, 1) CB(bp, i + 1, 1, 1) }
                    if (nx) { CB(bp, i + 1, 0, 2) CB(bp, i + 1, 1, 2) }
                    FM(0, 0) FM(0, 1)
                    if (nx) { SC(i + 1, 0) SC(i + 1, 1) SC(i + 1, 2) } else asm volatile("s_nop 3");
                    FM(1, 0) FM(1, 1)
                }
            }
        }
        PK(7, 0) PK(7, 1) PK(7, 2) DOT(1, 0) PK(7, 3) PK(7, 4) DOT(1, 1) PK(7, 5) PK(7, 6) DOT(1, 2)
        unsigned s2;
        asm volatile("v_mbcnt_lo_u32_b32 %0, -1, 0\n\tv_mbcnt_hi_u32_b32 %0, -1, %0" : "=v"(s2));
        DOT(1, 3)
        asm volatile("v_lshlrev_b32 %0, 1, %0\n\tv_and_b32 %0, 30, %0" : "+v"(s2));
        DOT(1, 4)
        unsigned s8 = s2 * 4u;
        double big = 0x1.8p52;
        PIN(s8); PIN(big);
        SB
        DOT(1, 5) DOT(1, 6)
        float v, v2;
        asm volatile("s_mov_b64 vcc, %6\n\ts_nop 6\n\tv_cndmask_b32 %0, %2, %3, vcc\n\tv_cndmask_b32 %1, %4, %5, vcc\n\tv_cndmask_b32 %0, %0, %1, %7"
                     : "=&v"(v), "=&v"(v2) : "v"(dg[0]), "v"(dg[1]), "v"(dg[2]), "v"(dg[3]), "s"(0xaaaaaaaaaaaaaaaaull), "s"(0xccccccccccccccccull) : "vcc");
        TE(c.tq[4])
        STAMP(4)
        TS(c.ts[14])
        if (__builtin_amdgcn_inverse_ballot_w64(0xf0000f0000f0000full)) deliver(c, v, 2 * nq + 1, s2, s8, big, sums, outp);
        const int ln = __builtin_amdgcn_mbcnt_hi(~0u, __builtin_amdgcn_mbcnt_lo(~0u, 0u));
        const int lane = ln, tid = w * 64 + ln;
        (void)lane; (void)tid;
        FINISH(c)
    }
}

extern "C" void kernel_launch(void* const* d_in, const int* in_sizes, int n_in,
                              void* d_out, int out_size, void* d_ws, size_t ws_size,
                              hipStream_t stream) {
    const int* idx = (const int*)d_in[0];
    const uint16_t* table = (const uint16_t*)d_in[1];
    const uint16_t* se_w1 = (const uint16_t*)d_in[2];
    const uint16_t* se_w2 = (const uint16_t*)d_in[3];
    const uint16_t* w_bi = (const uint16_t*)d_in[4];
    const uint16_t* w_cross = (const uint16_t*)d_in[5];
    const uint16_t* w_deep1 = (const uint16_t*)d_in[6];
    const uint16_t* w_deep2 = (const uint16_t*)d_in[7];
    (void)hipFuncSetAttribute((const void*)k_main, hipFuncAttributeMaxDynamicSharedMemorySize, LDS_T);
    k_main<<<256, 512, LDS_T, stream>>>(idx, table, w_bi, w_deep1, w_cross, (unsigned*)d_ws, se_w1, se_w2, w_deep2, (__hip_bfloat16*)d_out, (unsigned long long*)((char*)d_ws + (128ull << 20)));
}
```

```cpp
#include <hip/hip_runtime.h>
#include <hip/hip_bf16.h>
#include <stdint.h>

#define NF 26
#define CBAR 3
#define PREP 1
#define FBP 1
#define LK 0
#define WORD 0
#define NOPK 1
#define DPRI 0
#define DPRO 0
#define PRI(n) if (DPRI == (n)) __builtin_amdgcn_s_setprio(2); if (DPRO == (n)) __builtin_amdgcn_s_setprio(0);
#define NP 13
#define PROD_CLASS(slot) ((((slot) & 3) == 1) && (((slot) >> 2) & 1) == 0)
#define STAMP(k)
#define TS(x)
#define PREW 0
#define TW(k, x) if constexpr (PREW == k) { TS(x) }
#define TX(k)
#define TP(x)
#define TE(x) TS(x)
using bf16x8 = __attribute__((ext_vector_type(8))) short;
using f32x4 = __attribute__((ext_vector_type(4))) float;
using f32x2 = __attribute__((ext_vector_type(2))) float;
using s16x4 = __attribute__((ext_vector_type(4))) short;
using u32x4 = __attribute__((ext_vector_type(4))) unsigned;
using u32x8 = __attribute__((ext_vector_type(8))) unsigned;
using u32x2 = __attribute__((ext_vector_type(2))) unsigned;
using bf2 = __attribute__((ext_vector_type(2))) __bf16;
#define PIN(x) asm volatile("" : "+v"(x))
#define PINS(x) asm volatile("" : "+s"(x))
#define SB __builtin_amdgcn_sched_barrier(0);
#define CAT8(a, b) __builtin_shufflevector((a), (b), 0, 1, 2, 3, 4, 5, 6, 7)

#define SLOT 16384
#define OFF_B 65536
#define OFF_FL 131072
#define OFF_WBI (OFF_FL + 4096)
#define OFF_SW1 (OFF_WBI + 2048)
#define OFF_SW2 (OFF_SW1 + 1024)
#define OFF_WD2 (OFF_SW2 + 1024)
#define OFF_TAG (OFF_WD2 + 256)
#define OFF_NB (OFF_TAG + 768)
#define OFF_JNK (OFF_NB + 2048)
#define OFF_IDX (OFF_JNK + 2048)
#define OFF_RAW (OFF_IDX + 8192)
#define LDS_T (OFF_IDX + 16384)
constexpr int FOFF[4] = {OFF_B + SLOT, SLOT, OFF_B + 2 * SLOT, 2 * SLOT};
#define P_FLAG 3
#define P_POLL 6
#define P_CHK (P_POLL + 3)
#define P_STAT 9

#define WS_FLAG 1024
#define WS_SUM 4096
#define WS_SIDE 65536
#define WS_FRAG (1 << 20)

#define LDS(T, a) (*(__attribute__((address_space(3))) T*)(size_t)(a))
#define LDSV(a) (*(volatile const __attribute__((address_space(3))) unsigned*)(size_t)(a))
__device__ __forceinline__ unsigned pk_bf16(float lo, float hi) {
    const f32x2 v = {lo, hi};
    return __builtin_bit_cast(unsigned, __builtin_convertvector(v, bf2));
}
__device__ __forceinline__ unsigned cvt_o(float lo, float hi) { unsigned r; asm volatile("v_cvt_pk_bf16_f32 %0, %1, %2" : "=v"(r) : "v"(lo), "v"(hi)); return r; }
__device__ __forceinline__ float exp_o(float a) { float r; asm volatile("v_exp_f32 %0, %1" : "=v"(r) : "v"(a)); return r; }
template <typename A, typename B> __device__ __forceinline__ f32x4 mf0(const A& a, const B& b) { f32x4 d; asm volatile("v_mfma_f32_16x16x32_bf16 %0, %1, %2, 0" : "=v"(d) : "v"(a), "v"(b)); return d; }
template <typename A, typename B> __device__ __forceinline__ void mfa(f32x4& d, const A& a, const B& b) { asm volatile("v_mfma_f32_16x16x32_bf16 %0, %1, %2, %0" : "+v"(d) : "v"(a), "v"(b)); }
__device__ __forceinline__ float lo_f(unsigned x) { return __uint_as_float(x << 16); }
__device__ __forceinline__ float hi_f(unsigned x) { return __uint_as_float(x & 0xffff0000u); }
__device__ __forceinline__ s16x4 tr_read(unsigned a) {
    typedef short v4s __attribute__((ext_vector_type(4)));
    return __builtin_amdgcn_ds_read_tr16_b64_v4i16((__attribute__((address_space(3))) v4s*)(size_t)a);
}
#define DMA(name, ins) __device__ __forceinline__ void name(unsigned voff, const void* sbase, unsigned ldsaddr) { \
    asm volatile("s_mov_b32 m0, %2\n\ts_nop 0\n\t" ins :: "v"(voff), "s"(sbase), "s"(ldsaddr) : "memory"); }
DMA(dma16, "global_load_lds_dwordx4 %0, %1") DMA(dma4, "global_load_lds_dword %0, %1")
DMA(dma16sc, "global_load_lds_dwordx4 %0, %1 sc1") DMA(dma4sc, "global_load_lds_dword %0, %1 sc1")
__device__ __forceinline__ void st16sc(unsigned voff, void* sbase, u32x4 v) {
    asm volatile("global_store_dwordx4 %0, %1, %2 sc1\n\ts_nop 1" :: "v"(voff), "v"(v), "s"(sbase) : "memory");
}
template <int O> __device__ __forceinline__ void set_m0(unsigned base) { asm volatile("s_add_u32 m0, %0, %1" :: "s"(base), "n"(O) : "memory", "scc"); }
template <int O> __device__ __forceinline__ void bdma16o(unsigned voff, __amdgpu_buffer_rsrc_t rs, unsigned soff) {
    asm volatile("buffer_load_dwordx4 %0, %1, %2 offen offset:%3 lds" :: "v"(voff), "s"(rs), "s"(soff), "n"(O) : "memory");
}
template <int O> __device__ __forceinline__ void dma16o(unsigned voff, const void* sbase) {
    asm volatile("global_load_lds_dwordx4 %0, %1 offset:%2" :: "v"(voff), "s"(sbase), "n"(O) : "memory");
}
template <int O> __device__ __forceinline__ void dma16sco(unsigned voff, const void* sbase) {
    asm volatile("global_load_lds_dwordx4 %0, %1 offset:%2 sc1" :: "v"(voff), "s"(sbase), "n"(O) : "memory");
}
#define WAITCNT(vm, lgkm) __builtin_amdgcn_s_waitcnt(((vm) & 15) | (((vm) >> 4) << 14) | 0x70 | ((lgkm) << 8))
#define WAITX(vm, x, m) { WAITCNT((vm) + (x), 0); asm volatile("s_cmp_eq_u32 %0, 0\n\ts_cbranch_scc1 1f\n\ts_waitcnt vmcnt(%1)\n1:" :: "s"(m), "n"(vm) : "memory", "scc"); }
#define BARRIER asm volatile("s_barrier" ::: "memory")
__device__ __forceinline__ float sum32(float v) { const u32x2 p = __builtin_amdgcn_permlane32_swap(__float_as_uint(v), __float_as_uint(v), false, false); return __uint_as_float(p[0]) + __uint_as_float(p[1]); }
__device__ __forceinline__ float sum16(float v) { const u32x2 p = __builtin_amdgcn_permlane16_swap(__float_as_uint(v), __float_as_uint(v), false, false); return __uint_as_float(p[0]) + __uint_as_float(p[1]); }
__device__ __forceinline__ float sel(unsigned m, float a, float b) {
    return __uint_as_float(__builtin_amdgcn_bitop3_b32(m, __float_as_uint(a), __float_as_uint(b), 0xca));
}
__device__ __forceinline__ void wait_set(unsigned f, unsigned a) {
    if (__builtin_amdgcn_ballot_w64(f == 0u) != 0ull) {
#pragma unroll 1
        for (int it = 0; it < (1 << 20) && __builtin_amdgcn_ballot_w64(LDSV(a) == 0u) != 0ull; ++it) { }
    }
    asm volatile("" ::: "memory");
}

struct Com {
    char* ws;
    unsigned lane16, ldsb;
    int c, nq, rho, st;
};
typedef __attribute__((address_space(1))) char* gp;
__device__ __forceinline__ void deliver(const Com& c, float v, int part, unsigned s2, unsigned s8, double big, gp sums, gp outp) {
    const gp side = (gp)c.ws + (WS_SIDE + c.st * 2048 + c.c * 64);
    u32x2 mine = __builtin_bit_cast(u32x2, __builtin_fma((double)v, 0x1p42, big));
    mine[1] += 0x04004000u - 0x43380000u;
    const unsigned ab = __float_as_uint(v) << 1;
    if (__builtin_expect(ab - 0x61000000u >= 0x22000000u && ab != 0u, 0)) {
        mine = u32x2{0u, 0x04004000u + (0x40000u << part)};
        __hip_atomic_store((__attribute__((address_space(1))) float*)(side + part * 256 + s2 * 2), v, __ATOMIC_RELAXED, __HIP_MEMORY_SCOPE_AGENT);
        asm volatile("s_waitcnt vmcnt(0)" ::: "memory");
    }
    const unsigned long long mv = __builtin_bit_cast(unsigned long long, mine);
    const unsigned long long tot = mv + __hip_atomic_fetch_add((__attribute__((address_space(1))) unsigned long long*)(sums + s8), mv, __ATOMIC_RELAXED, __HIP_MEMORY_SCOPE_AGENT);
    const unsigned th = (unsigned)(tot >> 32);
    if (th >= 0x20000000u) {
        const u32x2 dv = {(unsigned)tot, (th & 0x3ffffu) | 0x43300000u};
        float r = (float)__builtin_fma(__builtin_bit_cast(double, dv), 0x1p-42, -0x1.2p10);
        if (__builtin_expect(th >= 0x20040000u, 0)) {
#pragma unroll 1
            for (int k = 0; k < 8; ++k)
                if ((th >> (18 + k)) & 1u) r += __hip_atomic_load((__attribute__((address_space(1))) float*)(side + k * 256 + s2 * 2), __ATOMIC_RELAXED, __HIP_MEMORY_SCOPE_AGENT);
        }
        *(__attribute__((address_space(1))) uint16_t*)(outp + s2) = (uint16_t)pk_bf16(r, r);
    }
}

struct Deep : Com {
    __amdgpu_buffer_rsrc_t rw1;
    const char* wsf; const char* wd2;
    unsigned sG[4], sB;
    unsigned m1, m3, m4;
    int unit;
    unsigned long long pmask, watch;
    unsigned tad, tag, pl;
    unsigned boff[4], ra[4], rb[4];
    float w2[16];
    f32x4 ac[4][4];
    bf16x8 fa[2][4];
    s16x4 lo[2][4], hi[2][4];
};
template <int Q> __device__ __forceinline__ void prepB(Deep& c) {
    if constexpr (Q == 0) c.sB = 24 * 32768; else c.sB = c.sG[(Q - 1) & 3] + ((Q - 1) >> 2) * (8 * 32768);
    PINS(c.sB);
    set_m0<OFF_B + (Q & 3) * SLOT>(c.ldsb);
}
template <int I> __device__ __forceinline__ void issueB(Deep& c) { bdma16o<I * 1024>(c.boff[I], c.rw1, c.sB); }
__device__ __forceinline__ void mkboff(Deep& d, int lane, int cw, int nq) {
    const unsigned b0 = (unsigned)(((cw & 1) * 64 + (cw >> 1) * 32 + (lane >> 3)) * 512 + nq * 128 + ((lane & 7) ^ ((lane >> 3) & 2)) * 16);
    d.boff[0] = b0; d.boff[1] = (b0 ^ 64u) + 3072; d.boff[2] = b0 + 6144; d.boff[3] = (b0 ^ 64u) + 9216;
#pragma unroll
    for (int k = 0; k < 4; ++k) PIN(d.boff[k]);
}
template <int P, int N> __device__ __forceinline__ void rd1(Deep& c) {
    constexpr int tab[12] = {0, 4, 5, 6, 7, 1, 2, 3, 8, 9, 10, 11}, k = tab[N < 12 ? N : 0];
    if constexpr (N >= 12) { }
    else if constexpr (k < 4) c.fa[P & 1][k] = LDS(const bf16x8, c.ra[k] + (P & 3) * SLOT);
    else if constexpr (k & 1) c.hi[P & 1][(k - 4) >> 1] = tr_read(c.rb[(k - 4) >> 1] + (P & 3) * SLOT + 512);
    else c.lo[P & 1][(k - 4) >> 1] = tr_read(c.rb[(k - 4) >> 1] + (P & 3) * SLOT);
    SB
}
template <int P, int M, int K> __device__ __forceinline__ void mk(Deep& c) {
    if constexpr (P >= 0) mfa(c.ac[M][K], CAT8(c.lo[P & 1][K], c.hi[P & 1][K]), c.fa[P & 1][M]);
    SB
}
template <int P, int N> __device__ __forceinline__ void m1(Deep& c) { mk<P, (N >> 1) & 3, (N >> 3) * 2 + (N & 1)>(c); }
template <int P, int M> __device__ __forceinline__ void m4(Deep& c) { mk<P, M, 0>(c); mk<P, M, 1>(c); mk<P, M, 2>(c); mk<P, M, 3>(c); }
#define DOPS 4, 4, 4, 4, 4, 4, 4, 0, 0, 8, 4, 2, 5, 5, 0, 4
#define UORD 10, 11, 12, 13, 14, 15, 16
constexpr int dops_[16] = {DOPS}, uord_[17] = {0, 1, 2, 3, 4, 5, 6, 7, 8, 9, UORD};
constexpr int dops(int S) { return dops_[S]; }
constexpr int dcum(int S) { int n = 12; for (int k = 0; k <= S; ++k) n += dops_[k]; return n; }
constexpr int place(int U) { int k = 0; while (uord_[k] != U) ++k; return k; }
static_assert(dcum(15) == 68 && dcum(0) == 16 && dcum(2) == 24 && dcum(6) == 40, "the single operations keep their places");
constexpr int dyoung(int S, int U) { return dcum(S) - 4 * (place(U) + 1) + (S == 1 || S == 2); }
static_assert(dyoung(10, 10) >= 0 && dyoung(11, 11) >= 0 && dyoung(12, 12) >= 0 && dyoung(13, 13) >= 0 && place(13) < place(14), "");
template <int S, int I> __device__ __forceinline__ void dissue1(Deep& c) {
    constexpr int n = dcum(S - 1) + I, U = uord_[n >> 2];
    static_assert(S >= (U < 15 ? U - 3 : U == 15 ? 13 : 15), "slot in use");
    if constexpr (U < NP) {
        if constexpr ((n & 3) == 0 || (I == 0 && S <= 9)) { prepB<U>(c); asm volatile("s_nop 0"); }
        issueB<n & 3>(c);
    } else {
        if constexpr ((n & 3) == 0) { set_m0<FOFF[U - NP]>(c.ldsb); asm volatile("s_nop 0"); }
        dma16sco<(n & 3) * 1024>(c.lane16, c.wsf + (U - NP) * 16384);
    }
    SB
}
template <int S, int I0, int I1> __device__ __forceinline__ void dissue(Deep& c) {
    if constexpr (I0 < I1 && I0 < dops(S)) { if constexpr (I0 >= 0) dissue1<S, I0>(c); dissue<S, I0 + 1, I1>(c); }
}
__device__ __forceinline__ unsigned tag_addr(int lane, int unit, int nq, int st, int xcd) {
    const unsigned l4 = (unsigned)lane * 4u;
    const unsigned a0 = (unsigned)(WS_FLAG + unit * 4) + l4 * 32u;
    const unsigned a1 = (((unsigned)(nq * 256) + l4 * 64u) & 768u) + (unsigned)(st * 4);
    const unsigned a2 = (unsigned)(WS_FLAG + xcd * 128 - 64) + l4;
    const unsigned lo = l4 < 32u ? a0 : a1, hi = l4 < 168u ? a2 : (unsigned)(st * 4);
    return l4 < 64u ? lo : hi;
}
__device__ __forceinline__ void publish(Deep& c, unsigned sv) {
    asm volatile("s_cmp_eq_u64 %3, 0\n\ts_cbranch_scc1 1f\n\ts_mov_b64 exec, %3\n\tglobal_store_dword %0, %1, %2 sc1\n\ts_mov_b64 exec, -1\n1:"
                 :: "v"(c.tad), "v"(sv), "s"(c.ws), "s"(c.pmask) : "memory", "scc");
}
__device__ __forceinline__ void produce(Deep& c) {
    if (c.m1 == 0u) {
        const int unit = c.unit;
        const unsigned p8 = (c.lane16 << 4) & 0x300u, e2 = (c.lane16 >> 5) & 0x1eu;
        const unsigned rb = (unsigned)(OFF_RAW + c.c * 32) + e2;
        const unsigned dm = p8 == (c.lane16 & 0x300u) ? ~0u : 0u;
        const int m = unit < 24 ? 2 * (unit & 6) + (unit & 1) + ((unit >> 2) & 2) : 4 * (unit & 1) + 2;
        unsigned fo = (unsigned)(WS_FRAG + c.c * 65536 + (unit >> 4) * 1024 + m * 64) + e2 * 2048u + ((c.lane16 & 0x30u) ^ (unsigned)((m & 4) << 3));
        if (unit >= 24) fo += c.lane16 & 0x200u;
#pragma unroll
        for (int pq = 0; pq < 2; ++pq) {
            u32x4 fr;
#pragma unroll
            for (int d = 0; d < 4; ++d) {
                const unsigned rp = rb + (d < 3 ? p8 : 0u) + (unsigned)(pq * (NF * 128) + 1024 * d);
                fr[d] = LDS(const uint16_t, rp) | ((unsigned)LDS(const uint16_t, rp + 128) << 16);
            }
            fr[3] &= dm;
            st16sc(fo + pq * 2048u, c.ws, fr);
        }
        if (unit >= 24) { st16sc(fo ^ 512u, c.ws, u32x4{0u, 0u, 0u, 0u}); st16sc((fo ^ 512u) + 2048u, c.ws, u32x4{0u, 0u, 0u, 0u}); }
    }
    SB
}
__device__ __forceinline__ void check_flags(Deep& c) {
    const unsigned long long open = __builtin_amdgcn_ballot_w64(c.tag == c.pl) & (c.nq ? 0x7ffffff0000ull : 0x3ffffff0000ull);
    if (__builtin_expect(open != 0ull, 0)) {
#pragma unroll 1
        for (int it = 0; it < (1 << 22); ++it) {
            const unsigned v = __hip_atomic_load((__attribute__((address_space(1))) unsigned*)((gp)c.ws + c.tad), __ATOMIC_RELAXED, __HIP_MEMORY_SCOPE_AGENT);
            if ((__builtin_amdgcn_ballot_w64(v == c.tag) & open) == 0ull) break;
            __builtin_amdgcn_s_sleep(2);
        }
    }
    asm volatile("" ::: "memory");
    SB
}
__device__ __forceinline__ void fetch_status(Deep& c) {
    if (c.m4 == 0u) {
        if (c.nq) dma16sc(c.lane16, c.ws, OFF_NB + 1024);
        else dma4sc(c.tad, c.ws, OFF_TAG + 512);
    }
    SB
}
constexpr int x3(int P) { return (P == P_FLAG || P == P_FLAG + 1) + (P == P_POLL || P == P_POLL + 1); }
#define TOP9 0
#define DTOPS 2, 2, 4, 2, 2, 2, 4, 2, 4, TOP9, 4, 4, 4
constexpr int dtop_[13] = {DTOPS};
constexpr int dtop(int P) { return dtop_[P]; }
template <int P, int N> __device__ __forceinline__ void dbody(Deep& c) {
    constexpr int top = dtop(P);
    m1<P - 1, N>(c);
    rd1<P, N>(c);
    if constexpr (N >= 12 && P < 8) dissue<P, top + N - 12, (N == 15 ? 16 : top + N - 11)>(c);
    if constexpr (N < 15) dbody<P, N + 1>(c);
}
template <int P, int N0, int N1> __device__ __forceinline__ void rdn(Deep& c) { if constexpr (N0 < N1) { rd1<P, N0>(c); rdn<P, N0 + 1, N1>(c); } }
#define GAP9 2
template <int P, int N> __device__ __forceinline__ void dnow(Deep& c) {
    m1<P, N>(c);
    if constexpr (P == 8) dissue<P, N - 16 + dops(P), N - 15 + dops(P)>(c);
    else if constexpr (N == 15) dissue<P, dtop(P) + N / GAP9, 16>(c);
    else if constexpr (N % GAP9 == GAP9 - 1) dissue<P, dtop(P) + N / GAP9, dtop(P) + N / GAP9 + 1>(c);
    if constexpr (N < 15) dnow<P, N + 1>(c);
}
#define WPOS 7
#define GAPL 3
constexpr int dbef(int S, int N) { const int k = (N + GAPL - 2) / GAPL; return N > 15 || k > dops(S) ? dops(S) : k; }
template <int S, int N> __device__ __forceinline__ void dgap(Deep& c) { dissue<S, dbef(S, N), dbef(S, N + 1)>(c); }
template <int P, int N> __device__ __forceinline__ void dlate(Deep& c) {
    m1<P - 1, N>(c);
    if constexpr (N < 4) rd1<P, (N ? N + 4 : 0)>(c);
    if constexpr (N >= WPOS && N < WPOS + 8) {
        if constexpr (N == WPOS) { WAITCNT(dyoung(P, P) + dbef(P + 1, N), 15); SB }
        rd1<P, (N < WPOS + 4 ? N - WPOS + 1 : N - WPOS + 4)>(c);
    }
    dgap<P + 1, N>(c);
    if constexpr (N < 15) dlate<P, N + 1>(c);
}
template <int P> struct DStep {
    static __device__ __forceinline__ void run(Deep& c) {
        TW(0, c.ts[P])
        SB
        if constexpr (P == P_FLAG) publish(c, c.tag + 1u);
        if constexpr (P == 1) { dma4((unsigned)(c.nq * 128) + ((c.lane16 >> 2) & 124u), c.wd2, OFF_WD2); SB }
        if constexpr (P < 8) dissue<P, 0, dtop(P)>(c);
        dbody<P, 0>(c);
        if constexpr (P == 4) {
#pragma unroll
            for (int k = 0; k < 4; ++k) {
                const u32x2 v = LDS(const u32x2, (unsigned)(OFF_WD2 + k * 32) + ((c.lane16 >> 5) & 24u));
                c.w2[4 * k] = lo_f(v[0]); c.w2[4 * k + 1] = hi_f(v[0]); c.w2[4 * k + 2] = lo_f(v[1]); c.w2[4 * k + 3] = hi_f(v[1]);
            }
#pragma unroll
            for (int k = 0; k < 16; ++k) PIN(c.w2[k]);
        }
        if constexpr (P == 8) { WAITCNT(63, 0); BARRIER; TX(1) dnow<8, 0>(c); TX(2) WAITX(dyoung(8, 9), 1, c.m3) rdn<9, 1, 5>(c); rdn<9, 8, 12>(c); }
        if constexpr (P == P_POLL) { if (c.m3 == 0u) dma4sc(c.tad, c.ws, OFF_TAG + 256); }
        SB
        TW(1, c.ts[P])
        if constexpr (P + 1 < P_FLAG && P < 2) WAITX(dyoung(P, P + 1), 2, c.m1)
        else if constexpr (x3(P) > 0) WAITX(dyoung(P, P + 1), x3(P), c.m3)
        else WAITCNT(dyoung(P, P + 1), P == 8 ? 15 : 0);
        TW(2, c.ts[P])
        BARRIER;
        if constexpr (P < 8) DStep<P + 1>::run(c);
    }
};
static_assert(P_CHK == 9 && P_STAT == 9, "");
__device__ __forceinline__ void dlast(Deep& c) {
    TS(c.ts[9])
    rd1<9, 0>(c); rdn<9, 5, 8>(c);
    c.pl = LDSV(OFF_TAG + 256 + (c.lane16 >> 2)); SB
    fetch_status(c);
    dissue<9, 0, dtop(9)>(c);
    TX(3)
    dnow<9, 0>(c);
    TX(4)
    check_flags(c);
    dissue<10, 0, 16>(c);
    c.watch = 0ull;
    if (c.m4 == 0u) {
        WAITCNT(dops(9) + dops(10), 15);
        asm volatile("" ::: "memory");
        if (c.nq) {
            const u32x4 e4 = LDS(const u32x4, OFF_NB + c.lane16), l4 = LDS(const u32x4, OFF_NB + 1024 + c.lane16);
            c.watch = __builtin_amdgcn_ballot_w64(e4[0] == l4[0] || e4[1] == l4[1] || e4[2] == l4[2] || e4[3] == l4[3]);
        } else c.watch = __builtin_amdgcn_ballot_w64(LDSV(OFF_TAG + 512 + (c.lane16 >> 2)) == c.tag) & 0xe00ull;
    }
    SB
    TX(5)
    WAITCNT(dyoung(10, 10), 15); SB
    rdn<10, 1, 5>(c); rdn<10, 8, 12>(c);
    TS(c.ts[10])
    BARRIER;
    TS(c.ts[11])
    rd1<10, 0>(c); rdn<10, 5, 8>(c);
    dissue<11, 0, 16>(c);
    dlate<11, 0>(c);
    TS(c.ts[12])
    dlate<12, 0>(c);
    TS(c.ts[13])
}

struct Cross : Com {
    const char* tb[4];
    unsigned ga[40];
    unsigned re[2], rx[2], rx0, six, wf[2];
    s16x4 wa[2];
    bf16x8 wb[2], wr[2];
    u32x4 fe[2][2][2];
    f32x4 ew[NF], zs[4];
    u32x4 X[NP], bp[4];
    float k0, k1, k2;
    unsigned am;
    u32x4 on[4], s1, zq;
};
template <int I> __device__ __forceinline__ void issueA(Cross& c, unsigned off) { dma16o<I * 1024>(off, c.tb[I]); }
template <int K, int S, int N> __device__ __forceinline__ void sw1(Cross& c) {
    constexpr int a = 1 + 4 * K + (S == 1 ? 2 : (S == 3 ? 1 : 0)), b = 1 + 4 * K + (S == 0 ? 1 : (S == 2 ? 2 : 3)), Q = N >> 2, R = N & 3;
    float x = c.ew[2 * a + Q][R], y = c.ew[2 * b + Q][R];
    if (S >= 2) asm volatile("v_permlane32_swap_b32 %0, %1" : "+v"(x), "+v"(y)); else asm volatile("v_permlane16_swap_b32 %0, %1" : "+v"(x), "+v"(y));
    c.ew[2 * a + Q][R] = x; c.ew[2 * b + Q][R] = y;
}
template <int K, int S, int N0 = 0, int N1 = 8> __device__ __forceinline__ void swn(Cross& c) { if constexpr (N0 < N1) { sw1<K, S, N0>(c); swn<K, S, N0 + 1, N1>(c); } }
constexpr int sw0(int P) { return P <= 3 ? 0 : P <= 5 ? 8 : P == 6 ? 20 : P == 7 ? 32 : 40; }
template <int P, int N0, int N1> __device__ __forceinline__ void tsw(Cross& c) {
    if constexpr (N0 < N1 && sw0(P) + N0 < sw0(P + 1)) { sw1<((sw0(P) + N0) >> 5), ((sw0(P) + N0) >> 3) & 3, (sw0(P) + N0) & 7>(c); tsw<P, N0 + 1, N1>(c); }
}
constexpr int fbuf(int P) { return P & 1; }
template <int P, int N> __device__ __forceinline__ void cmm(Cross& c) {
    if constexpr (P >= 0) {
        constexpr int B = fbuf(P);
        if constexpr (N < 2)
            asm volatile("v_smfmac_f32_16x16x64_bf16 %0, %1, %2, %3" : "+v"(c.zs[P ? (P - 1) >> 2 : 3])
                         : "v"(c.on[P ? (P - 1) & 3 : 0]), "v"(__builtin_shufflevector(c.fe[B][N][0], c.fe[B][N][1], 0, 1, 2, 3, 4, 5, 6, 7)), "v"(c.six));
        else if constexpr (N < 4) c.ew[2 * P + (N & 1)] = mf0(P ? c.wb[0] : c.wr[0], c.fe[B][0][N & 1]);
        else mfa(c.ew[2 * P + (N & 1)], P ? c.wb[1] : c.wr[1], c.fe[B][1][N & 1]);
    }
    SB
}
template <int P, int N> __device__ __forceinline__ void rdE(Cross& c) {
    c.fe[fbuf(P)][N >> 1][N & 1] = LDS(const u32x4, c.re[N >> 1] + (P & 3) * SLOT + (N & 1) * 2048);
    SB
}
template <int K> __device__ __forceinline__ void zpack(Cross& c) {
    float a = c.zs[K][0], b = c.zs[K][2];
    unsigned r;
    asm volatile("v_add_f32 %1, %1, %3\n\tv_add_f32 %2, %2, %4\n\tv_cvt_pk_bf16_f32 %0, %1, %2" : "=v"(r), "+v"(a), "+v"(b) : "v"(c.zs[K][1]), "v"(c.zs[K][3]));
    c.zq[K] = r;
    SB
}
template <int K> __device__ __forceinline__ void zclear(Cross& c) { c.zs[K] = f32x4{0.f, 0.f, 0.f, 0.f}; PIN(c.zs[K]); SB }
template <int P> __device__ __forceinline__ void rdX(Cross& c) {
    constexpr int k = 1 + ((P - 1) & ~3);
    if constexpr (P == 0) c.X[0] = LDS(const u32x4, c.rx0);
    else if constexpr ((P & 3) == 2)
        asm volatile("s_mov_b32 exec_hi, 0\n\tds_read_b128 %0, %4\n\tds_read_b128 %1, %4 offset:2048\n\tds_read_b128 %2, %5\n\tds_read_b128 %3, %5 offset:2048\n\ts_mov_b32 exec_hi, -1"
                     : "=&v"(c.X[k]), "=&v"(c.X[k + 1]), "=&v"(c.X[k + 2]), "=&v"(c.X[k + 3]) : "v"(c.rx[0]), "v"(c.rx[1]) : "memory");
    else if constexpr ((P & 3) == 0)
        asm volatile("s_mov_b32 exec_lo, 0\n\tds_read_b128 %0, %4\n\tds_read_b128 %1, %4 offset:2048\n\tds_read_b128 %2, %5\n\tds_read_b128 %3, %5 offset:2048\n\ts_mov_b32 exec_lo, -1"
                     : "+v"(c.X[k]), "+v"(c.X[k + 1]), "+v"(c.X[k + 2]), "+v"(c.X[k + 3]) : "v"(c.rx[0]), "v"(c.rx[1]) : "memory");
    SB
}
#define B8 1
#define G8 1
template <int P> struct CStep {
    static __device__ __forceinline__ void run(Cross& c) {
        TW(0, c.ts[P])
        SB
        cmm<P - 1, 2>(c); rdE<P, 0>(c);
        cmm<P - 1, 3>(c); rdE<P, 1>(c);
        cmm<P - 1, 4>(c); rdE<P, 2>(c);
        cmm<P - 1, 5>(c); rdE<P, 3>(c);
        cmm<P - 1, 0>(c);
        if constexpr (P + 3 < NP) set_m0<((P + 3) & 3) * SLOT>(c.ldsb);
        cmm<P - 1, 1>(c);
        rdX<P>(c);
        constexpr bool xw = P && !(P & 1);
#define GA(J) if constexpr (P + 3 < NP) issueA<J>(c, c.ga[4 * P + (J)]); SB
        if constexpr (P == 4 || P == 8) zclear<P / 4>(c);
        if constexpr (P == 2 || P == 6) zpack<(P == 2 ? 3 : 0)>(c);
        if constexpr (P == 8) {
            WAITCNT(63, 0); SB
            if constexpr (B8 == 0) BARRIER;
            cmm<8, 2>(c); cmm<8, 3>(c); GA(0) cmm<8, 4>(c); cmm<8, 5>(c); GA(1) cmm<8, 0>(c); cmm<8, 1>(c);
#pragma unroll
            for (int r = 0; r < 4; ++r) { c.bp[r][3] = cvt_o(c.ew[0][r], c.ew[1][r]); c.bp[r][0] = cvt_o(c.ew[2][r], c.ew[3][r]); }
            c.k0 = -2.0f * 0.7978845608028654f * 1.4426950408889634f / 64.0f; c.k1 = c.k0 * (0.044715f / 4096.0f); c.k2 = -64.0f / 1.4426950408889634f;
            c.am = (c.wf[0] & 16u) ? ~0u : 0u;
            PIN(c.k0); PIN(c.k1); PIN(c.k2); PIN(c.am);
            SB
            if constexpr (B8 == 1) BARRIER;
#define GB(J) issueA<J>(c, c.ga[36 + (J)]); SB
            if constexpr (G8 == 0) {
                swn<1, 1>(c); zpack<1>(c); swn<1, 2>(c); swn<1, 3>(c);
#pragma unroll
                for (int r = 0; r < 4; ++r) c.bp[r][1] = cvt_o(c.ew[10][r], c.ew[11][r]);
                SB
                GA(2) GA(3) set_m0<0>(c.ldsb); asm volatile("s_nop 0"); GB(0) GB(1) GB(2) GB(3)
            } else {
                swn<1, 1>(c); zpack<1>(c); GA(2) swn<1, 2, 0, 4>(c); GA(3) set_m0<0>(c.ldsb); swn<1, 2, 4, 8>(c); GB(0) swn<1, 3, 0, 4>(c); GB(1) swn<1, 3, 4, 8>(c); GB(2)
#pragma unroll
                for (int r = 0; r < 4; ++r) c.bp[r][1] = cvt_o(c.ew[10][r], c.ew[11][r]);
                SB
                GB(3)
            }
        }
        else if constexpr (xw) { tsw<P, 0, 4>(c); WAITCNT(63, 0); GA(0) tsw<P, 4, 6>(c); GA(1) tsw<P, 6, 8>(c); GA(2) tsw<P, 8, 10>(c); GA(3) tsw<P, 10, 12>(c); }
        else { GA(0) GA(1) tsw<P, 0, 4>(c); GA(2) tsw<P, 4, 8>(c); GA(3) tsw<P, 8, 12>(c); }
        if constexpr (P == 3) {
            const unsigned r6 = c.lane16 * 4u + (unsigned)c.rho * 256u;
            const unsigned am = (c.lane16 >> 8) + (unsigned)c.rho;
            c.wf[0] = (r6 & 0x3c0u) | (((am << 4) ^ ((r6 >> 3) & 0x20u)) & 0x30u) | SLOT;
            c.wf[1] = c.wf[0] + OFF_B;
            const unsigned wap = OFF_SW2 + (c.lane16 & 0x3c0u) + ((c.lane16 >> 1) & 0x18u);
            c.wa[0] = tr_read(wap); c.wa[1] = tr_read(wap + 32);
            const unsigned r2 = (c.lane16 >> 3) & 30u, s1 = OFF_SW1 + min(r2, 24u), sg = s1 + (am & 3u) * 52u;
#pragma unroll
            for (int k = 0; k < 4; ++k) {
                const unsigned a = (k < 3 ? sg : s1) + k * 208;
                const unsigned v = LDS(const uint16_t, a) | ((unsigned)LDS(const uint16_t, a + 26) << 16);
                c.s1[k] = r2 < 26u ? v : 0u;
            }
            PIN(c.s1);
            PIN(c.wf[0]); PIN(c.wf[1]); PIN(c.wa[0]); PIN(c.wa[1]);
        }
        SB
        TW(1, c.ts[P])
        if constexpr (P == 8) { TX(0) }
        WAITCNT(P == 8 ? 12 : 8, 0);
        TW(2, c.ts[P])
        BARRIER;
        if constexpr (P < 8) CStep<P + 1>::run(c);
    }
};

#define PFC 142
#define WPRE 6
__global__ __launch_bounds__(512) void k_main(const int* __restrict__ idx, const uint16_t* __restrict__ table,
        const uint16_t* __restrict__ w_bi, const uint16_t* __restrict__ w_deep1, const uint16_t* __restrict__ w_cross,
        unsigned* __restrict__ ws32, const uint16_t* __restrict__ se_w1, const uint16_t* __restrict__ se_w2,
        const uint16_t* __restrict__ w_deep2_, __hip_bfloat16* __restrict__ out_, unsigned long long* __restrict__ stamps) {
    SB
    const int tid = threadIdx.x, lane = tid & 63;
    const int w = __builtin_amdgcn_readfirstlane(tid >> 6);
    const int bid = blockIdx.x;
    const int xcd = bid & 7, slot = bid >> 3;
    const int st = xcd * 8 + (slot >> 2), nq = slot & 3;
    const int s0 = st * 64;
    const int rho = (st + nq) & 3, cw = w & 3;
    const int l15 = lane & 15, g = lane >> 4;
    LDS(u32x2, OFF_FL + tid * 8) = u32x2{0u, 0u};
    unsigned long long pc;
    {
        const unsigned ln = min((unsigned)(4 + w * 4 + (lane & 3) + ((lane >> 2) & 1) * (32 + nq * 32)), (unsigned)PFC) * 128u;
        asm volatile("s_getpc_b64 %0\n\ts_mov_b32 m0, %2\n\ts_mov_b64 exec, 0xff\n\tglobal_load_lds_dword %1, %0\n\ts_mov_b64 exec, -1"
                     : "=&s"(pc) : "v"(ln), "n"(OFF_JNK) : "memory");
    }
    SB
    float gc0 = -2.0f * 0.7978845608028654f * 1.4426950408889634f, gc1 = gc0 * 0.044715f;
#define FIN_ST(c)
#define FIN_SA(c)
#define FINISH(c) STAMP(5) FIN_ST(c) FIN_SA(c) __builtin_amdgcn_endpgm(); __builtin_unreachable();
    unsigned lane16 = (unsigned)lane * 16u;
    int deep = w >> 2;
    Deep d;
    d.ldsb = (unsigned)(cw * 4096);
    unsigned o2 = min(lane16, 624u), im0 = (unsigned)(OFF_IDX + cw * 2048), io = (unsigned)((s0 + 16 * cw) * (NF * 4));
    mkboff(d, lane, cw, nq);
#pragma unroll
    for (int k = 0; k < 4; ++k) { d.sG[k] = (unsigned)(((k + rho) & 3) * (2 * 32768)); PINS(d.sG[k]); }
    d.unit = xcd + 8 * ((st & 7) >> 1);
    d.m1 = (unsigned)((nq ^ 1) | (st & 1) | ((d.unit + 32 - NF) >> 5));
    PIN(lane16); PIN(o2); PINS(im0); PINS(io); PINS(deep); PINS(d.ldsb); PINS(d.m1);
    asm volatile("s_mov_b32 m0, %0" :: "s"(im0) : "memory");
    SB
    asm volatile("" :: "s"(idx), "s"(table), "s"(se_w1), "s"(se_w2), "s"(w_bi), "s"(w_deep1), "s"(w_cross), "s"(ws32));
    asm volatile("s_cmp_lg_u32 %3, 0\n\ts_cbranch_scc1 1f\n\tglobal_load_lds_dwordx4 %0, %2\n\tglobal_load_lds_dwordx4 %1, %2 offset:1024\n1:"
                 :: "v"(lane16), "v"(o2), "s"((const char*)idx + io), "s"(deep) : "memory", "scc");
    SB
    typedef const __attribute__((address_space(4))) unsigned long long* kap;
    kap ka = (kap)__builtin_amdgcn_kernarg_segment_ptr();
    PINS(ka);
    d.wd2 = (const char*)ka[8];
    const gp out = (gp)ka[9];
    unsigned long long w1p;
    asm volatile("s_mov_b64 %0, %1" : "=s"(w1p) : "s"(w_deep1));
    d.rw1 = __builtin_amdgcn_make_buffer_rsrc((void*)w1p, (short)0, NF * 32768, 0x00020000);
    if (deep) {
        asm volatile("s_mov_b32 m0, %2\n\ts_nop 0\n\tbuffer_load_dword %0, %1, 0 offen lds" :: "v"(min(lane16, 80u) * 8u + (unsigned)((bid * 4 + cw) * 768)), "s"(d.rw1), "n"(OFF_JNK) : "memory");
    }
    prepB<0>(d);
    TS(tz1)
    BARRIER;
    TS(tz2)
    Cross x;
    x.c = cw; x.nq = nq; x.rho = rho; x.st = st; x.lane16 = lane16; x.ws = (char*)ws32; x.ldsb = d.ldsb;
    unsigned srcA, srcB, ib[4];
    u32x2 ia[NP], ic[NP];
    if (deep) {
        issueB<0>(d); issueB<1>(d); issueB<2>(d); issueB<3>(d);
        SB
        dma16((unsigned)((cw & 1) * 4096 + (lane >> 1) * 128 + nq * 32 + (lane & 1) * 16), w_bi, OFF_WBI + (cw & 1) * 1024);
        dma4((unsigned)min(cw * 64 + lane, 168) * 4u, se_w1, OFF_SW1 + cw * 256);
        const int k = (l15 & 1) | ((l15 >> 2) & 2), r = 4 * cw + g;
        dma4((unsigned)((r < 13 ? r : 12) * (NF * 2) + k * 16 + (k == 3 ? 0 : ((l15 + 2 * rho) & 6) * 2)), se_w2, OFF_SW2 + cw * 256);
        SB
        if (__builtin_expect(d.m1 == 0u, 1)) {
            const unsigned r0 = (unsigned)(16 * cw + (lane >> 3)), lo = (lane16 & 0x70u) + (unsigned)(d.unit * 128);
            dma16(min(r0, 2u * NF - 1) * (NF * 128) + lo, w_cross, OFF_RAW + cw * 2048);
            dma16(min(r0 + 8u, 2u * NF - 1) * (NF * 128) + lo, w_cross, OFF_RAW + cw * 2048 + 1024);
            dma4sc((lane16 >> 2) + (unsigned)(cw * 256), ws32, OFF_NB + cw * 256);
        }
        if (cw == 3) dma4sc(tag_addr(lane, d.unit, nq, st, xcd), ws32, OFF_TAG);
        if ((cw ^ 2) + nq == 0 && lane < 32) st16sc(lane16, (char*)ws32 + WS_SUM + st * 512, u32x4{0u, 0u, 0u, 0u});
        SB
        if (nq != 3) { prepB<1>(d); asm volatile("s_nop 0"); issueB<0>(d); issueB<1>(d); issueB<2>(d); issueB<3>(d); }
        SB
    } else {
        Cross& c = x;
        srcA = (unsigned)(((lane & 7) ^ g) * 16); srcB = srcA ^ 64u;
        unsigned ib0 = (unsigned)(OFF_IDX + cw * 2048 + (lane >> 3) * 104);
#pragma unroll
        for (int k = 0; k < 4; ++k) { ib[k] = ib0 + (unsigned)(((k + rho) & 3) * 8); PIN(ib[k]); c.tb[k] = (const char*)table - k * 1024; PINS(c.tb[k]); }
        PIN(srcA); PIN(srcB); PIN(ib0);
        set_m0<0>(c.ldsb);
        SB
        WAITCNT(0, 15);
        asm volatile("" ::: "memory");
        STAMP(1)
        TP(c.tq[0])
        ia[0] = LDS(const u32x2, ib0 + 96); ic[0] = LDS(const u32x2, ib0 + 96 + 832);
#pragma unroll
        for (int q = 1; q < 3; ++q) { ia[q] = LDS(const u32x2, ib[q - 1]); ic[q] = LDS(const u32x2, ib[q - 1] + 832); }
        SB
#define GATHER(q) issueA<0>(c, ia[q][0] * 128u + srcA); issueA<1>(c, ic[q][0] * 128u + srcB); issueA<2>(c, ia[q][1] * 128u + srcA); issueA<3>(c, ic[q][1] * 128u + srcB);
        GATHER(0)
        set_m0<SLOT>(c.ldsb);
        SB
        GATHER(1)
        set_m0<2 * SLOT>(c.ldsb);
        TP(c.tq[1])
    }
    TS(tz3)
    int crs = deep ^ 1;
    PINS(crs); PINS(deep);
    if (crs == 0) {
        Deep& c = d;
        int lane;
        asm volatile("v_mbcnt_lo_u32_b32 %0, -1, 0\n\tv_mbcnt_hi_u32_b32 %0, -1, %0" : "=v"(lane));
        const int l15 = lane & 15, g = lane >> 4, tid = w * 64 + lane;
        c.c = cw; c.nq = nq; c.rho = rho; c.st = st; c.lane16 = (unsigned)lane * 16u; c.ws = (char*)ws32;
        c.m3 = (unsigned)(cw ^ 3); c.m4 = c.m3 | min(c.m1, (unsigned)nq);
        c.tad = tag_addr(lane, c.unit, nq, st, xcd);
        PIN(c.tad);
        SB
        mkboff(d, lane, cw, nq);
        {
            const unsigned a0 = (unsigned)(cw * 4096 + (cw & 1) * 2048 + l15 * 128 + ((4 * (cw >> 1) + g) ^ (l15 >> 1)) * 16);
            const int q = l15 >> 2, p = l15 & 3;
            const unsigned b0 = (unsigned)(OFF_B + cw * 4096 + (8 * g + q) * 128 + ((p >> 1) ^ (q & 2) ^ ((g & 1) << 2)) * 16 + (p & 1) * 8);
#pragma unroll
            for (int k = 0; k < 4; ++k) { c.ra[k] = a0 ^ (unsigned)(k * 4096); c.rb[k] = b0 ^ (unsigned)(k * 32); PIN(c.ra[k]); PIN(c.rb[k]); }
#pragma unroll
            for (int k = 0; k < 16; ++k) { c.ac[k >> 2][k & 3] = f32x4{0.f, 0.f, 0.f, 0.f}; PIN(c.ac[k >> 2][k & 3]); }
        }
        unsigned exw[4], fl = (c.lane16 >> 2) + (unsigned)(OFF_FL + cw * 256), fr = c.lane16 + OFF_FL, s2 = c.lane16 >> 3, s8 = c.lane16 >> 1;
#pragma unroll
        for (int k = 0; k < 4; ++k) { exw[k] = c.lane16 + (unsigned)(OFF_B + (cw ^ k) * 4096); PIN(exw[k]); }
        double big = 0x1.8p52;
        float va[2] = {0.f, 0.f};
        const unsigned dl = (unsigned)(l15 - 4 * g), on = (dl & 1u) ? 0x3f800000u : 0x3f80u;
        u32x4 unit = {dl < 2u ? on : 0u, dl - 2u < 2u ? on : 0u, dl < 2u ? on : 0u, dl - 2u < 2u ? on : 0u};
        PIN(fl); PIN(fr); PIN(s2); PIN(s8); PIN(big); PIN(gc0); PIN(gc1); PIN(va[0]); PIN(va[1]); PIN(unit);
        c.wsf = c.ws + WS_FRAG + nq * 65536 + cw * 4096;
        c.pmask = cw != 3 ? 0ull : (c.m1 == 0u ? 0x1ffull : 0x100ull);
        PINS(c.pmask); PINS(c.m3); PINS(c.m4); PINS(c.wsf);
        if (nq == 3) { prepB<1>(c); asm volatile("s_nop 0"); issueB<0>(c); issueB<1>(c); issueB<2>(c); issueB<3>(c); }
        TP(c.tq[0])
        WAITCNT(4, 0); BARRIER;
        TP(c.tq[1])
        prepB<2>(c); asm volatile("s_nop 0"); issueB<0>(c); issueB<1>(c); issueB<2>(c); issueB<3>(c);
        TP(c.tq[2])
        produce(c);
        c.tag = LDSV(OFF_TAG + (c.lane16 >> 2));
        PIN(c.tag);
        TP(c.tq[3])
        BARRIER;
        TP(c.tq[4])
        gp sums = (gp)c.ws + (unsigned)(WS_SUM + (s0 + 16 * cw) * 8), outp = out + (unsigned)((s0 + 16 * cw) * 2);
        PINS(sums); PINS(outp);
        DStep<0>::run(c);
        dlast(c);
        u32x4 px[3][2];
#define EXC(e) { _Pragma("unroll") for (int k = 0; k < 8; ++k) px[(e) - 1][k >> 2][k & 3] = cvt_o(c.ac[e][k >> 1][2 * (k & 1)], c.ac[e][k >> 1][2 * (k & 1) + 1]); SB }
#define EXA(e, k) (((e) == 3 ? 3 * SLOT : ((e) - 1) * 2048) + (k) * 1024)
#define EXW(e) { LDS(u32x4, exw[0] + EXA(e, 0)) = px[(e) - 1][0]; LDS(u32x4, exw[0] + EXA(e, 1)) = px[(e) - 1][1]; SB }
#define EX2(e, k) { px[(e) - 1][(k) >> 1][2 * ((k) & 1)] = cvt_o(c.ac[e][k][0], c.ac[e][k][1]); px[(e) - 1][(k) >> 1][2 * ((k) & 1) + 1] = cvt_o(c.ac[e][k][2], c.ac[e][k][3]); SB }
#define FBAR(n, N) if ((n) == FBP) { TE(c.tq[0]) WAITCNT(dyoung(13, 13) + dbef(14, N), 15); TX(6) BARRIER; SB PRI(1) }
#define MG(m, k, N) mk<12, m, k>(c); dgap<14, N>(c);
        const unsigned long long watch = c.watch;
        FBAR(0, 0)
        MG(3, 0, 0) MG(3, 1, 1) MG(3, 2, 2) MG(3, 3, 3) MG(2, 0, 4) MG(2, 1, 5) MG(2, 2, 6) MG(2, 3, 7)
        FBAR(1, 8)
        MG(1, 0, 8) MG(1, 1, 9) EX2(3, 0) MG(1, 2, 10) EX2(3, 1) MG(1, 3, 11) EX2(3, 2)
        MG(0, 0, 12) EX2(3, 3) MG(0, 1, 13) EX2(2, 0) MG(0, 2, 14) EX2(2, 1) MG(0, 3, 15) EX2(2, 2)
        FBAR(2, 16)
        EX2(2, 3)
        if (!WORD) EXC(1)
        FBAR(3, 16)
        PRI(2)
        WAITCNT(0, 15); SB
        LDS(unsigned, fl) = fl; SB
        EXW(3) EXW(2)
        if (WORD) EXC(1)
        EXW(1)
        TE(c.tq[1])
        SB
        PRI(9)
        LDS(unsigned, fl + 1024) = fl;
        SB
        dissue<15, 0, 16>(c);
        publish(c, c.tag + 2u);
        TE(c.tq[2])
#pragma unroll 1
        for (int it = 0; it < (1 << 20) && __builtin_amdgcn_ballot_w64(LDSV(fr + 1024) == 0u) != 0ull; ++it) { }
        asm volatile("" ::: "memory");
        TE(c.tq[3])
        PRI(3)
        u32x2 pa[4], pb[4];
#pragma unroll
        for (int k = 0; k < 2; ++k) px[2][k] = LDS(const u32x4, exw[3] + EXA(3, k));
#pragma unroll
        for (int k = 0; k < 4; ++k) {
            pa[k] = *(volatile const __attribute__((address_space(3))) u32x2*)(size_t)(exw[1] + EXA(1, k >> 1) + (k & 1) * 8);
            pb[k] = *(volatile const __attribute__((address_space(3))) u32x2*)(size_t)(exw[2] + EXA(2, k >> 1) + (k & 1) * 8);
        }
        SB
#pragma unroll
        for (int k = 0; k < 4; ++k)
            asm volatile("v_mfma_f32_16x16x16_bf16 %0, %1, %2, %0" : "+v"(c.ac[0][k]) : "v"(u32x2{unit[0], unit[1]}), "v"(u32x2{px[2][k >> 1][2 * (k & 1)], px[2][k >> 1][2 * (k & 1) + 1]}));
        asm volatile("s_nop 2");
#pragma unroll
        for (int k = 0; k < 4; ++k) mfa(c.ac[0][k], unit, u32x4{pa[k][0], pa[k][1], pb[k][0], pb[k][1]});
        asm volatile("s_nop 4");
#pragma unroll
        for (int k = 0; k < 4; ++k) {
            PRI(4 + k)
#define LAST(n) if ((n) == LK) { WAITX(0, 1, c.m3) LDS(unsigned, fl + 2048) = fl; SB TX(0) }
            LAST(2 * k)
            float x[4] = {c.ac[0][k][0], c.ac[0][k][1], c.ac[0][k][2], c.ac[0][k][3]}, y[4];
            asm volatile("v_mul_f32 %0, %4, %4\n\tv_mul_f32 %1, %5, %5\n\tv_mul_f32 %2, %6, %6\n\tv_mul_f32 %3, %7, %7\n\t"
                         "v_fma_f32 %0, %0, %9, %8\n\tv_fma_f32 %1, %1, %9, %8\n\tv_fma_f32 %2, %2, %9, %8\n\tv_fma_f32 %3, %3, %9, %8\n\t"
                         "v_mul_f32 %0, %0, %4\n\tv_mul_f32 %1, %1, %5\n\tv_mul_f32 %2, %2, %6\n\tv_mul_f32 %3, %3, %7\n\t"
                         "v_exp_f32 %0, %0\n\tv_exp_f32 %1, %1\n\tv_exp_f32 %2, %2\n\tv_exp_f32 %3, %3"
                         : "=&v"(y[0]), "=&v"(y[1]), "=&v"(y[2]), "=&v"(y[3]) : "v"(x[0]), "v"(x[1]), "v"(x[2]), "v"(x[3]), "v"(gc0), "v"(gc1));
            LAST(2 * k + 1)
            asm volatile("v_mul_f32 %0, %0, %10\n\tv_mul_f32 %1, %1, %11\n\tv_mul_f32 %2, %2, %12\n\tv_mul_f32 %3, %3, %13\n\t"
                         "v_add_f32 %4, 1.0, %4\n\tv_add_f32 %5, 1.0, %5\n\tv_add_f32 %6, 1.0, %6\n\tv_add_f32 %7, 1.0, %7\n\t"
                         "v_rcp_f32 %4, %4\n\tv_rcp_f32 %5, %5\n\tv_rcp_f32 %6, %6\n\tv_rcp_f32 %7, %7\n\t"
                         "v_fmac_f32 %8, %0, %4\n\tv_fmac_f32 %9, %1, %5\n\tv_fmac_f32 %8, %2, %6\n\tv_fmac_f32 %9, %3, %7"
                         : "+v"(x[0]), "+v"(x[1]), "+v"(x[2]), "+v"(x[3]), "+v"(y[0]), "+v"(y[1]), "+v"(y[2]), "+v"(y[3]), "+v"(va[0]), "+v"(va[1])
                         : "v"(c.w2[4 * k]), "v"(c.w2[4 * k + 1]), "v"(c.w2[4 * k + 2]), "v"(c.w2[4 * k + 3]));
        }
        TE(c.tq[4])
        const float v = sum16(sum32(va[0] + va[1]));
        TS(c.ts[14])
        if (__builtin_amdgcn_inverse_ballot_w64(0xffffull)) deliver(c, v, 2 * nq, s2, s8, big, sums, outp);
        if (__builtin_expect(watch != 0ull, 0)) {
            const u32x4 e4 = LDS(const u32x4, OFF_NB + c.lane16), l4 = LDS(const u32x4, OFF_NB + 1024 + c.lane16);
            bool opn[4];
#pragma unroll
            for (int k = 0; k < 4; ++k) opn[k] = c.m1 == 0u && e4[k] == l4[k];
            bool own = nq == 0 && lane >= 9 && lane < 12 && LDSV(OFF_TAG + 512 + lane * 4) == c.tag;
            unsigned f = c.tag + 3u;
#pragma unroll 1
            for (int it = 0; it < (1 << 22) && __builtin_amdgcn_ballot_w64(opn[0] || opn[1] || opn[2] || opn[3] || own) != 0ull; ++it) {
                bool chg = false;
#pragma unroll
                for (int k = 0; k < 4; ++k)
                    if (opn[k] && __hip_atomic_load(ws32 + lane * 4 + k, __ATOMIC_RELAXED, __HIP_MEMORY_SCOPE_AGENT) != e4[k]) { opn[k] = false; chg = true; }
                if (own && __hip_atomic_load(ws32 + (c.tad >> 2), __ATOMIC_RELAXED, __HIP_MEMORY_SCOPE_AGENT) != c.tag) { own = false; chg = true; }
                if (__builtin_amdgcn_ballot_w64(chg) != 0ull) { publish(c, f); ++f; }
                else __builtin_amdgcn_s_sleep(2);
            }
        }
        FINISH(c)
    } else {
        Cross& c = x;
#pragma unroll
        for (int q = 3; q < NP; ++q) { ia[q] = LDS(const u32x2, ib[(q - 1) & 3] + ((q - 1) >> 2) * 32); ic[q] = LDS(const u32x2, ib[(q - 1) & 3] + ((q - 1) >> 2) * 32 + 832); }
#pragma unroll
        for (int q = 3; q < NP; ++q) {
            c.ga[4 * q - 12] = ia[q][0] * 128u + srcA; c.ga[4 * q - 11] = ic[q][0] * 128u + srcB;
            c.ga[4 * q - 10] = ia[q][1] * 128u + srcA; c.ga[4 * q - 9] = ic[q][1] * 128u + srcB;
        }
#pragma unroll
        for (int k = 0; k < 40; ++k) PIN(c.ga[k]);
        {
            const int G = (g + rho) & 3;
            const unsigned ea = (unsigned)(cw * 4096 + l15 * 128);
            c.re[0] = ea + (unsigned)((g ^ (l15 >> 1)) * 16); c.re[1] = c.re[0] ^ 64u;
            c.rx[0] = ea + (unsigned)(((g + 1) & 3) * SLOT + ((2 * nq) ^ (l15 >> 1)) * 16); c.rx[1] = c.rx[0] ^ 16u;
            c.rx0 = ea + (unsigned)((G & 1) * 2048 + ((2 * nq + (G >> 1)) ^ (l15 >> 1)) * 16);
            c.six = (l15 & 1) ? 0xeeeeu : 0x4444u;
#pragma unroll
            for (int k = 0; k < 4; ++k) { const unsigned o = (l15 >> 1) == 2 * k + (g >> 1) ? 0x3f803f80u : 0u; c.on[k] = u32x4{o, o, o, o}; PIN(c.on[k]); }
            PIN(c.re[0]); PIN(c.re[1]); PIN(c.rx[0]); PIN(c.rx[1]); PIN(c.rx0); PIN(c.six);
            zclear<3>(c); zclear<0>(c);
        }
        SB
        BARRIER;
        GATHER(2)
        TP(c.tq[2])
        {
            const int q = (lane >> 2) & 3, p = lane & 3;
            const unsigned a0 = (unsigned)(OFF_WBI + (8 * g + q) * 32 + p * 8), a1 = (unsigned)(OFF_WBI + (8 * g + q) * 32 + ((p + rho) & 3) * 8);
#pragma unroll
            for (int k = 0; k < 2; ++k) {
                c.wb[k] = CAT8(tr_read(a0 + k * 1024), tr_read(a0 + k * 1024 + 128));
                c.wr[k] = CAT8(tr_read(a1 + k * 1024), tr_read(a1 + k * 1024 + 128));
            }
#pragma unroll
            for (int k = 0; k < 2; ++k) { PIN(c.wb[k]); PIN(c.wr[k]); }
        }
        gp sums = (gp)c.ws + (unsigned)(WS_SUM + (s0 + 16 * cw) * 8), outp = out + (unsigned)((s0 + 16 * cw) * 2);
        PINS(sums); PINS(outp);
        TP(c.tq[3])
        WAITCNT(8, 0); BARRIER;
        TP(c.tq[4])
        __builtin_amdgcn_s_setprio(1);
        CStep<0>::run(c);
#define RDE(P) rdE<P, 0>(c); rdE<P, 1>(c); rdE<P, 2>(c); rdE<P, 3>(c);
        TS(c.ts[9])
        RDE(9)
        cmm<9, 2>(c); cmm<9, 3>(c); cmm<9, 4>(c); cmm<9, 5>(c); cmm<9, 0>(c); cmm<9, 1>(c);
        TX(1) WAITCNT(8, 15); SB TX(2)
        RDE(10)
        cmm<10, 2>(c); cmm<10, 3>(c); cmm<10, 4>(c); cmm<10, 5>(c);
        rdX<10>(c);
        cmm<10, 0>(c); cmm<10, 1>(c);
        TX(3) WAITCNT(4, 0); SB
        RDE(11)
        swn<2, 0>(c);
        cmm<11, 2>(c); cmm<11, 3>(c); cmm<11, 4>(c); cmm<11, 5>(c); cmm<11, 0>(c); cmm<11, 1>(c);
        WAITCNT(0, 15); SB
        TS(c.ts[10])
        RDE(12)
        if (CBAR == 0) { BARRIER; TS(c.ts[11]) }
        u32x4 bq[4];
        u32x4 (&bp)[4] = c.bp;
        f32x2 T[6][8];
#define SPLIT(s0, s1, i0, i1) { _Pragma("unroll") for (int s = s0; s < s1; ++s) _Pragma("unroll") for (int i_ = i0; i_ < i1; ++i_) { \
            const f32x4 x = c.ew[2 * (1 + 4 * (s >> 1) + (i_ >> 1)) + (s & 1)]; T[s][i_] = f32x2{x[2 * (i_ & 1)], x[2 * (i_ & 1) + 1]}; } }
#define CB(B, i, h, k) { B[(2 * (i) + (h)) & 3][k] = cvt_o(T[2 * (k)][i][h], T[2 * (k) + 1][i][h]); }
#define SC(i, k) { asm volatile("v_pk_mul_f32 %0, %1, %0 op_sel_hi:[0,1]" : "+v"(T[2 * (k)][i]) : "v"(ao[k])); asm volatile("v_pk_mul_f32 %0, %1, %0 op_sel:[1,0]" : "+v"(T[2 * (k) + 1][i]) : "v"(ao[k])); }
        SPLIT(0, 4, 0, 8)
        SB
        cmm<12, 0>(c); cmm<12, 1>(c);
        STAMP(2)
        TS(c.ts[12])
        cmm<12, 2>(c); cmm<12, 3>(c);
        rdX<12>(c);
        if (CBAR == 1) { BARRIER; TS(c.ts[11]) }
        zpack<2>(c);
        cmm<12, 4>(c); cmm<12, 5>(c);
        const f32x4 hp = mf0(c.s1, c.zq);
        if (CBAR == 2) { BARRIER; TS(c.ts[11]) }
        asm volatile("s_nop 6");
        swn<2, 1, 0, 2>(c);
        float hq[4];
        asm volatile("v_mul_f32 %0, %4, %4\n\tv_mul_f32 %1, %5, %5\n\tv_mul_f32 %2, %6, %6\n\tv_mul_f32 %3, %7, %7\n\t"
                     "v_fma_f32 %0, %0, %9, %8\n\tv_fma_f32 %1, %1, %9, %8\n\tv_fma_f32 %2, %2, %9, %8\n\tv_fma_f32 %3, %3, %9, %8\n\t"
                     "v_mul_f32 %0, %0, %4\n\tv_mul_f32 %1, %1, %5\n\tv_mul_f32 %2, %2, %6\n\tv_mul_f32 %3, %3, %7\n\t"
                     "v_exp_f32 %0, %0\n\tv_exp_f32 %1, %1\n\tv_exp_f32 %2, %2\n\tv_exp_f32 %3, %3\n\t"
                     "v_fma_f32 %0, %0, %10, %10\n\tv_fma_f32 %1, %1, %10, %10\n\tv_fma_f32 %2, %2, %10, %10\n\tv_fma_f32 %3, %3, %10, %10\n\t"
                     "v_rcp_f32 %0, %0\n\tv_rcp_f32 %1, %1\n\tv_rcp_f32 %2, %2\n\tv_rcp_f32 %3, %3\n\t"
                     "v_mul_f32 %0, %0, %4\n\tv_mul_f32 %1, %1, %5\n\tv_mul_f32 %2, %2, %6\n\tv_mul_f32 %3, %3, %7\n\t"
                     "v_cvt_pk_bf16_f32 %0, %0, %1\n\tv_cvt_pk_bf16_f32 %1, %2, %3"
                     : "=&v"(hq[0]), "=&v"(hq[1]), "=&v"(hq[2]), "=&v"(hq[3]) : "v"(hp[0]), "v"(hp[1]), "v"(hp[2]), "v"(hp[3]), "v"(c.k0), "v"(c.k1), "v"(c.k2));
        if (CBAR == 3) { BARRIER; TS(c.ts[11]) }
        const f32x2 hb = {hq[0], hq[1]};
        swn<2, 1, 2, 4>(c);
        f32x4 av[2];
        asm volatile("v_mfma_f32_16x16x16_bf16 %0, %1, %2, 0" : "=&v"(av[0]) : "v"(c.wa[0]), "v"(hb));
        asm volatile("v_mfma_f32_16x16x16_bf16 %0, %1, %2, 0" : "=&v"(av[1]) : "v"(c.wa[1]), "v"(hb));
        swn<2, 1, 4, 8>(c);
        swn<2, 2, 0, 4>(c);
        TS(c.ts[13])
        SB
        float as[8];
#pragma unroll
        for (int r = 0; r < 8; ++r) as[r] = exp_o(av[r >> 2][r & 3]);
        asm volatile("v_add_f32 %0, 1.0, %0\n\tv_add_f32 %1, 1.0, %1\n\tv_add_f32 %2, 1.0, %2\n\tv_add_f32 %3, 1.0, %3\n\tv_add_f32 %4, 1.0, %4\n\tv_add_f32 %5, 1.0, %5\n\tv_add_f32 %6, 1.0, %6\n\tv_add_f32 %7, 1.0, %7\n\t"
                     "v_rcp_f32 %0, %0\n\tv_rcp_f32 %1, %1\n\tv_rcp_f32 %2, %2\n\tv_rcp_f32 %3, %3\n\tv_rcp_f32 %4, %4\n\tv_rcp_f32 %5, %5\n\tv_rcp_f32 %6, %6\n\tv_rcp_f32 %7, %7"
                     : "+v"(as[0]), "+v"(as[1]), "+v"(as[2]), "+v"(as[3]), "+v"(as[4]), "+v"(as[5]), "+v"(as[6]), "+v"(as[7]));
        unsigned flg;
        SB
        swn<2, 2, 4, 8>(c);
        SPLIT(4, 6, 0, 2) SPLIT(4, 6, 4, 6)
#define WSEQ(n) LDS(const u32x4, c.wf[(((n) >> 4) & 1) ^ 1] + ((n) >> 5) * SLOT + ((n) & 15) * 1024)
        u32x4 wq[64];
#define FIRST { TE(c.tq[0]) SB WAITCNT(63, 0); BARRIER; TX(6) SB _Pragma("unroll") for (int r = 0; r < WPRE; ++r) wq[r] = WSEQ(r); SB }
        if (!PREP) FIRST
        CB(bp, 0, 0, 2) CB(bp, 0, 1, 2)
#define MU(x, a) { float x0_ = x[0], x1_ = x[1]; asm volatile("v_mul_f32 %0, %2, %0\n\tv_mul_f32 %1, %2, %1" : "+v"(x0_), "+v"(x1_) : "v"(a)); x = f32x2{x0_, x1_}; }
        f32x2 ao[4] = {{as[0], as[1]}, {as[2], as[3]}, {as[4], as[5]}, {as[6], as[7]}};
        {
            f32x2 m[4] = {{c.ew[0][0], c.ew[0][1]}, {c.ew[1][0], c.ew[1][1]}, {c.ew[0][2], c.ew[0][3]}, {c.ew[1][2], c.ew[1][3]}};
#pragma unroll
            for (int r = 0; r < 4; r += 2) {
                if (NOPK) { MU(m[r], as[6]) MU(m[r + 1], as[7]) }
                else {
                    asm volatile("v_pk_mul_f32 %0, %1, %0 op_sel_hi:[0,1]" : "+v"(m[r]) : "v"(ao[3]));
                    asm volatile("v_pk_mul_f32 %0, %1, %0 op_sel:[1,0]" : "+v"(m[r + 1]) : "v"(ao[3]));
                }
            }
            if (NOPK) { _Pragma("unroll") for (int r = 0; r < 6; ++r) MU(T[r][0], as[r]) }
            else { SC(0, 0) SC(0, 1) SC(0, 2) }
#pragma unroll
            for (int r = 0; r < 4; ++r) bq[r][3] = cvt_o(m[r & 2][r & 1], m[(r & 2) + 1][r & 1]);
        }
        ao[3][0] = sel(c.am, as[7], as[6]);
        PIN(ao[3]);
        SB
        if (PREP) { FIRST swn<2, 3>(c); SB }
        TE(c.tq[1])
        f32x4 dg;
        f32x4 u[2][4];
        f32x2 sv[2][4];
        u32x4 pk[7];
#define FM(h, tl) { _Pragma("unroll") for (int q = 0; q < 2; ++q) asm volatile("v_pk_fma_f32 %0, %1, %2, %3" : "=v"(sv[h][2 * (tl) + q]) : "v"(f32x2{u[h][2 + (tl)][2 * q], u[h][2 + (tl)][2 * q + 1]}), "v"(ao[2 * (tl) + q]), "v"(f32x2{u[h][tl][2 * q], u[h][tl][2 * q + 1]})); }
#define PK(i, s) { pk[s][(i) & 3] = cvt_o(sv[0][(s) >> 1][(s) & 1], sv[1][(s) >> 1][(s) & 1]); }
#define DOT(eh, s) { const u32x4 x_ = (s) < 6 ? c.X[1 + 4 * ((s) >> 1) + ((s) & 1) + 2 * (eh)] : c.X[0]; if ((eh) || (s)) mfa(dg, pk[s], x_); else dg = mf0(pk[s], x_); }
#pragma unroll
        for (int i = 0; i < 8; ++i) {
            static_assert(dops(15) == 4, "");
            constexpr int f3 = OFF_FL - OFF_B - SLOT, f5 = OFF_FL + 2048 - OFF_B - SLOT;
            if (i == 2) { TE(c.tq[2]) }
            if (i == 4) { TE(c.tq[3]) }
            if (i == 1) { SPLIT(4, 6, 2, 4) SPLIT(4, 6, 6, 8) }
#pragma unroll
            for (int j = 0; j < 8; ++j) {
                const int n = 8 * i + j, h = j >> 2;
                const bool nx = i < 7, pv = i >= 1;
                SB
                if (!(j & 1)) { if (n + WPRE <= 64) WAITCNT(63, WPRE - 2); SB }
                u[h][j & 3] = mf0(wq[n], (j & 2) ? bq[(2 * i + h) & 3] : bp[(2 * i + h) & 3]);
                SB
                if (n == 5) flg = LDSV(c.wf[1] + f3);
                if (n + WPRE == 16) { SB TX(5) wait_set(flg, c.wf[1] + f3); SB TX(4) }
                if (n == 37 && f3 != f5) flg = LDSV(c.wf[1] + f5);
                if (n + WPRE == 48 && f3 != f5) { SB wait_set(flg, c.wf[1] + f5); SB }
                if (n + WPRE < 64) wq[n + WPRE] = WSEQ(n + WPRE);
                SB
                if (i == 0 && !PREP) { if (j == 2) swn<2, 3, 0, 1>(c); if (j == 3) swn<2, 3, 1, 2>(c); if (j == 4) swn<2, 3, 2, 4>(c); if (j == 5) swn<2, 3, 4, 5>(c); if (j == 6) swn<2, 3, 5, 7>(c); if (j == 7) swn<2, 3, 7, 8>(c); }
                if (j == 0) { CB(bq, i, 0, 0) if (i == 5) DOT(0, 4) CB(bq, i, 0, 1) }
                if (j == 1) { CB(bq, i, 0, 2) if (i == 5) DOT(0, 5) CB(bq, i, 1, 0) CB(bq, i, 1, 1) }
                if (j == 2) { CB(bq, i, 1, 2) if (i == 5) DOT(0, 6) if (pv) PK(i - 1, 0) }
                if (j == 3) { if (pv) { PK(i - 1, 1) if (i == 4) DOT(0, 0) PK(i - 1, 2) } }
                if (j == 4) { if (pv) { PK(i - 1, 3) if (i == 4) DOT(0, 1) PK(i - 1, 4) } }
                if (j == 5) { if (pv) { PK(i - 1, 5) if (i == 4) DOT(0, 2) PK(i - 1, 6) } }
                if (j == 6) { if (nx && i) { CB(bp, i + 1, 0, 0) if (i == 4) DOT(0, 3) CB(bp, i + 1, 1, 0) } }
                if (j == 7) {
                    if (nx && i) { CB(bp, i + 1, 0, 1) CB(bp, i + 1, 1, 1) }
                    if (nx) { CB(bp, i + 1, 0, 2) CB(bp, i + 1, 1, 2) }
                    FM(0, 0) FM(0, 1)
                    if (nx) { SC(i + 1, 0) SC(i + 1, 1) SC(i + 1, 2) } else asm volatile("s_nop 3");
                    FM(1, 0) FM(1, 1)
                }
            }
        }
        PK(7, 0) PK(7, 1) PK(7, 2) DOT(1, 0) PK(7, 3) PK(7, 4) DOT(1, 1) PK(7, 5) PK(7, 6) DOT(1, 2)
        unsigned s2;
        asm volatile("v_mbcnt_lo_u32_b32 %0, -1, 0\n\tv_mbcnt_hi_u32_b32 %0, -1, %0" : "=v"(s2));
        DOT(1, 3)
        asm volatile("v_lshlrev_b32 %0, 1, %0\n\tv_and_b32 %0, 30, %0" : "+v"(s2));
        DOT(1, 4)
        unsigned s8 = s2 * 4u;
        double big = 0x1.8p52;
        PIN(s8); PIN(big);
        SB
        DOT(1, 5) DOT(1, 6)
        float v, v2;
        asm volatile("s_mov_b64 vcc, %6\n\ts_nop 6\n\tv_cndmask_b32 %0, %2, %3, vcc\n\tv_cndmask_b32 %1, %4, %5, vcc\n\tv_cndmask_b32 %0, %0, %1, %7"
                     : "=&v"(v), "=&v"(v2) : "v"(dg[0]), "v"(dg[1]), "v"(dg[2]), "v"(dg[3]), "s"(0xaaaaaaaaaaaaaaaaull), "s"(0xccccccccccccccccull) : "vcc");
        TE(c.tq[4])
        STAMP(4)
        TS(c.ts[14])
        if (__builtin_amdgcn_inverse_ballot_w64(0xf0000f0000f0000full)) deliver(c, v, 2 * nq + 1, s2, s8, big, sums, outp);
        const int ln = __builtin_amdgcn_mbcnt_hi(~0u, __builtin_amdgcn_mbcnt_lo(~0u, 0u));
        const int lane = ln, tid = w * 64 + ln;
        (void)lane; (void)tid;
        FINISH(c)
    }
}

extern "C" void kernel_launch(void* const* d_in, const int* in_sizes, int n_in,
                              void* d_out, int out_size, void* d_ws, size_t ws_size,
                              hipStream_t stream) {
    const int* idx = (const int*)d_in[0];
    const uint16_t* table = (const uint16_t*)d_in[1];
    const uint16_t* se_w1 = (const uint16_t*)d_in[2];
    const uint16_t* se_w2 = (const uint16_t*)d_in[3];
    const uint16_t* w_bi = (const uint16_t*)d_in[4];
    const uint16_t* w_cross = (const uint16_t*)d_in[5];
    const uint16_t* w_deep1 = (const uint16_t*)d_in[6];
    const uint16_t* w_deep2 = (const uint16_t*)d_in[7];
    (void)hipFuncSetAttribute((const void*)k_main, hipFuncAttributeMaxDynamicSharedMemorySize, LDS_T);
    k_main<<<256, 512, LDS_T, stream>>>(idx, table, w_bi, w_deep1, w_cross, (unsigned*)d_ws, se_w1, se_w2, w_deep2, (__hip_bfloat16*)d_out, (unsigned long long*)((char*)d_ws + (128ull << 20)));
}
```

```cpp
#include <hip/hip_runtime.h>
#include <hip/hip_bf16.h>
#include <stdint.h>

#define NF 26
#define CBAR 3
#define PREP 1
#define FBP 1
#define LK 0
#define WORD 0
#define NOPK 1
#define DPRI 0
#define DPRO 0
#define PRI(n) if (DPRI == (n)) __builtin_amdgcn_s_setprio(2); if (DPRO == (n)) __builtin_amdgcn_s_setprio(0);
#define NP 13
#define T3N 0
#define T3S 2
#define T30 512
#define T3P 0
#define PB1 1
#define PEAR 1
#define W2P 3
#define C3E 0
#define IW 1
#define PROD_CLASS(slot) ((((slot) & 3) == 1) && (((slot) >> 2) & 1) == 0)
#define STAMP(k)
#define TS(x)
#define PREW 0
#define TW(k, x) if constexpr (PREW == k) { TS(x) }
#define TX(k)
#define TP(x)
#define TE(x) TS(x)
using bf16x8 = __attribute__((ext_vector_type(8))) short;
using f32x4 = __attribute__((ext_vector_type(4))) float;
using f32x8 = __attribute__((ext_vector_type(8))) float;
using f32x2 = __attribute__((ext_vector_type(2))) float;
using s16x4 = __attribute__((ext_vector_type(4))) short;
using u32x4 = __attribute__((ext_vector_type(4))) unsigned;
using u32x8 = __attribute__((ext_vector_type(8))) unsigned;
using u32x2 = __attribute__((ext_vector_type(2))) unsigned;
using bf2 = __attribute__((ext_vector_type(2))) __bf16;
#define PIN(x) asm volatile("" : "+v"(x))
#define PINS(x) asm volatile("" : "+s"(x))
#define SB __builtin_amdgcn_sched_barrier(0);
#define CAT8(a, b) __builtin_shufflevector((a), (b), 0, 1, 2, 3, 4, 5, 6, 7)

#define SLOT 16384
#define OFF_B 65536
#define OFF_FL 131072
#define OFF_WBI (OFF_FL + 4096)
#define OFF_SW1 (OFF_WBI + 2048)
#define OFF_SW2 (OFF_SW1 + 1024)
#define OFF_WD2 (OFF_SW2 + 1024)
#define OFF_TAG (OFF_WD2 + 256)
#define OFF_NB (OFF_TAG + 768)
#define OFF_JNK (OFF_NB + 2048)
#define OFF_IDX (OFF_JNK + 2048)
#define OFF_RAW (OFF_IDX + 8192)
#define LDS_T (OFF_IDX + 16384)
constexpr int FOFF[4] = {OFF_B + SLOT, SLOT, OFF_B + 2 * SLOT, 2 * SLOT};
#define P_FLAG 3
#define P_POLL 6
#define P_CHK (P_POLL + 3)
#define P_STAT 9

#define WS_FLAG 1024
#define WS_SUM 4096
#define WS_SIDE 65536
#define WS_FRAG (1 << 20)

#define LDS(T, a) (*(__attribute__((address_space(3))) T*)(size_t)(a))
#define LDSV(a) (*(volatile const __attribute__((address_space(3))) unsigned*)(size_t)(a))
__device__ __forceinline__ unsigned pk_bf16(float lo, float hi) {
    const f32x2 v = {lo, hi};
    return __builtin_bit_cast(unsigned, __builtin_convertvector(v, bf2));
}
__device__ __forceinline__ unsigned cvt_o(float lo, float hi) { unsigned r; asm volatile("v_cvt_pk_bf16_f32 %0, %1, %2" : "=v"(r) : "v"(lo), "v"(hi)); return r; }
__device__ __forceinline__ float mul_o(float a, float b) { float r; asm volatile("v_mul_f32 %0, %1, %2" : "=v"(r) : "v"(a), "v"(b)); return r; }
__device__ __forceinline__ float add_o(float a, float b) { float r; asm volatile("v_add_f32 %0, %1, %2" : "=v"(r) : "v"(a), "v"(b)); return r; }
__device__ __forceinline__ float exp_o(float a) { float r; asm volatile("v_exp_f32 %0, %1" : "=v"(r) : "v"(a)); return r; }
template <typename A, typename B> __device__ __forceinline__ f32x4 mf0(const A& a, const B& b) { f32x4 d; asm volatile("v_mfma_f32_16x16x32_bf16 %0, %1, %2, 0" : "=v"(d) : "v"(a), "v"(b)); return d; }
template <typename A, typename B> __device__ __forceinline__ void mfa(f32x4& d, const A& a, const B& b) { asm volatile("v_mfma_f32_16x16x32_bf16 %0, %1, %2, %0" : "+v"(d) : "v"(a), "v"(b)); }
__device__ __forceinline__ float lo_f(unsigned x) { return __uint_as_float(x << 16); }
__device__ __forceinline__ float hi_f(unsigned x) { return __uint_as_float(x & 0xffff0000u); }
__device__ __forceinline__ s16x4 tr_read(unsigned a) {
    typedef short v4s __attribute__((ext_vector_type(4)));
    return __builtin_amdgcn_ds_read_tr16_b64_v4i16((__attribute__((address_space(3))) v4s*)(size_t)a);
}
#define DMA(name, ins) __device__ __forceinline__ void name(unsigned voff, const void* sbase, unsigned ldsaddr) { \
    asm volatile("s_mov_b32 m0, %2\n\ts_nop 0\n\t" ins :: "v"(voff), "s"(sbase), "s"(ldsaddr) : "memory"); }
DMA(dma16, "global_load_lds_dwordx4 %0, %1") DMA(dma4, "global_load_lds_dword %0, %1")
DMA(dma16sc, "global_load_lds_dwordx4 %0, %1 sc1") DMA(dma4sc, "global_load_lds_dword %0, %1 sc1")
__device__ __forceinline__ void st16sc(unsigned voff, void* sbase, u32x4 v) {
    asm volatile("global_store_dwordx4 %0, %1, %2 sc1\n\ts_nop 1" :: "v"(voff), "v"(v), "s"(sbase) : "memory");
}
template <int O> __device__ __forceinline__ void set_m0(unsigned base) { asm volatile("s_add_u32 m0, %0, %1" :: "s"(base), "n"(O) : "memory", "scc"); }
template <int O> __device__ __forceinline__ void bdma16o(unsigned voff, __amdgpu_buffer_rsrc_t rs, unsigned soff) {
    asm volatile("buffer_load_dwordx4 %0, %1, %2 offen offset:%3 lds" :: "v"(voff), "s"(rs), "s"(soff), "n"(O) : "memory");
}
template <int O> __device__ __forceinline__ void dma16o(unsigned voff, const void* sbase) {
    asm volatile("global_load_lds_dwordx4 %0, %1 offset:%2" :: "v"(voff), "s"(sbase), "n"(O) : "memory");
}
template <int O> __device__ __forceinline__ void dma16sco(unsigned voff, const void* sbase) {
    asm volatile("global_load_lds_dwordx4 %0, %1 offset:%2 sc1" :: "v"(voff), "s"(sbase), "n"(O) : "memory");
}
#define WAITCNT(vm, lgkm) __builtin_amdgcn_s_waitcnt(((vm) & 15) | (((vm) >> 4) << 14) | 0x70 | ((lgkm) << 8))
#define WAITX(vm, x, m) { WAITCNT((vm) + (x), 0); asm volatile("s_cmp_eq_u32 %0, 0\n\ts_cbranch_scc1 1f\n\ts_waitcnt vmcnt(%1)\n1:" :: "s"(m), "n"(vm) : "memory", "scc"); }
#define BARRIER asm volatile("s_barrier" ::: "memory")
#define SCO 1
#define SPC 0
#define SPD 6
#define SCOUT(n) if (SCO & (n)) asm volatile("s_mov_b64 exec, 0\n\ts_branch .Lspot" #n "\n.Lback" #n ":\n\ts_mov_b64 exec, -1" ::: "memory");
#define SPOT(n) { if (SCO & (n)) asm volatile(".Lspot" #n ":\n\ts_cbranch_execz .Lback" #n ::: "memory"); else asm volatile(".Lspot" #n ":" ::: "memory"); }
#define PADTO(n) ".Lpa" #n ":\n\t.fill ((0 - (.Lpa" #n " - k_front) - (.Lspot" #n " - .Lpb" #n ")) & 4095) / 4, 4, 0xbf800000\n.Lpb" #n ":"
__device__ __forceinline__ float sum32(float v) { const u32x2 p = __builtin_amdgcn_permlane32_swap(__float_as_uint(v), __float_as_uint(v), false, false); return __uint_as_float(p[0]) + __uint_as_float(p[1]); }
__device__ __forceinline__ float sum16(float v) { const u32x2 p = __builtin_amdgcn_permlane16_swap(__float_as_uint(v), __float_as_uint(v), false, false); return __uint_as_float(p[0]) + __uint_as_float(p[1]); }
__device__ __forceinline__ float sel(unsigned m, float a, float b) {
    return __uint_as_float(__builtin_amdgcn_bitop3_b32(m, __float_as_uint(a), __float_as_uint(b), 0xca));
}
__device__ __forceinline__ void wait_set(unsigned f, unsigned a) {
    if (__builtin_amdgcn_ballot_w64(f == 0u) != 0ull) {
#pragma unroll 1
        for (int it = 0; it < (1 << 20) && __builtin_amdgcn_ballot_w64(LDSV(a) == 0u) != 0ull; ++it) { }
    }
    asm volatile("" ::: "memory");
}

struct Com {
    char* ws;
    unsigned lane16, ldsb;
    int c, nq, rho, st;
};
typedef __attribute__((address_space(1))) char* gp;
__device__ __forceinline__ void deliver(const Com& c, float v, int part, unsigned s2, unsigned s8, double big, gp sums, gp outp) {
    const gp side = (gp)c.ws + (WS_SIDE + c.st * 2048 + c.c * 64);
    u32x2 mine = __builtin_bit_cast(u32x2, __builtin_fma((double)v, 0x1p42, big));
    mine[1] += 0x04004000u - 0x43380000u;
    const unsigned ab = __float_as_uint(v) << 1;
    if (__builtin_expect(ab - 0x61000000u >= 0x22000000u && ab != 0u, 0)) {
        mine = u32x2{0u, 0x04004000u + (0x40000u << part)};
        __hip_atomic_store((__attribute__((address_space(1))) float*)(side + part * 256 + s2 * 2), v, __ATOMIC_RELAXED, __HIP_MEMORY_SCOPE_AGENT);
        asm volatile("s_waitcnt vmcnt(0)" ::: "memory");
    }
    const unsigned long long mv = __builtin_bit_cast(unsigned long long, mine);
    const unsigned long long tot = mv + __hip_atomic_fetch_add((__attribute__((address_space(1))) unsigned long long*)(sums + s8), mv, __ATOMIC_RELAXED, __HIP_MEMORY_SCOPE_AGENT);
    const unsigned th = (unsigned)(tot >> 32);
    if (th >= 0x20000000u) {
        const u32x2 dv = {(unsigned)tot, (th & 0x3ffffu) | 0x43300000u};
        float r = (float)__builtin_fma(__builtin_bit_cast(double, dv), 0x1p-42, -0x1.2p10);
        if (__builtin_expect(th >= 0x20040000u, 0)) {
#pragma unroll 1
            for (int k = 0; k < 8; ++k)
                if ((th >> (18 + k)) & 1u) r += __hip_atomic_load((__attribute__((address_space(1))) float*)(side + k * 256 + s2 * 2), __ATOMIC_RELAXED, __HIP_MEMORY_SCOPE_AGENT);
        }
        *(__attribute__((address_space(1))) uint16_t*)(outp + s2) = (uint16_t)pk_bf16(r, r);
    }
}

struct Deep : Com {
    __amdgpu_buffer_rsrc_t rw1;
    const char* wsf; const char* wd2;
    unsigned sG[4], sB;
    unsigned m1, m3, m4;
    int unit;
    unsigned long long pmask;
    unsigned tad, tag, pl;
    unsigned boff[4], ra[4], rb[4];
    float w2[16];
    f32x4 ac[4][4];
    bf16x8 fa[2][4];
    s16x4 lo[2][4], hi[2][4];
};
template <int Q> __device__ __forceinline__ void prepB(Deep& c) {
    if constexpr (Q == 0) c.sB = 24 * 32768; else c.sB = c.sG[(Q - 1) & 3] + ((Q - 1) >> 2) * (8 * 32768);
    PINS(c.sB);
    set_m0<OFF_B + (Q & 3) * SLOT>(c.ldsb);
}
template <int I> __device__ __forceinline__ void issueB(Deep& c) { bdma16o<I * 1024>(c.boff[I], c.rw1, c.sB); }
__device__ __forceinline__ void mkboff(Deep& d, int lane, int cw, int nq) {
    const unsigned b0 = (unsigned)(((cw & 1) * 64 + (cw >> 1) * 32 + (lane >> 3)) * 512 + nq * 128 + ((lane & 7) ^ ((lane >> 3) & 2)) * 16);
    d.boff[0] = b0; d.boff[1] = (b0 ^ 64u) + 3072; d.boff[2] = b0 + 6144; d.boff[3] = (b0 ^ 64u) + 9216;
#pragma unroll
    for (int k = 0; k < 4; ++k) PIN(d.boff[k]);
}
template <int P, int N> __device__ __forceinline__ void rd1(Deep& c) {
    constexpr int tab[12] = {0, 4, 5, 6, 7, 1, 2, 3, 8, 9, 10, 11}, k = tab[N < 12 ? N : 0];
    if constexpr (N >= 12) { }
    else if constexpr (k < 4) c.fa[P & 1][k] = LDS(const bf16x8, c.ra[k] + (P & 3) * SLOT);
    else if constexpr (k & 1) c.hi[P & 1][(k - 4) >> 1] = tr_read(c.rb[(k - 4) >> 1] + (P & 3) * SLOT + 512);
    else c.lo[P & 1][(k - 4) >> 1] = tr_read(c.rb[(k - 4) >> 1] + (P & 3) * SLOT);
    SB
}
template <int P, int M, int K> __device__ __forceinline__ void mk(Deep& c) {
    if constexpr (P >= 0) mfa(c.ac[M][K], CAT8(c.lo[P & 1][K], c.hi[P & 1][K]), c.fa[P & 1][M]);
    SB
}
template <int P, int N> __device__ __forceinline__ void m1(Deep& c) { mk<P, (N >> 1) & 3, (N >> 3) * 2 + (N & 1)>(c); }
template <int P, int M> __device__ __forceinline__ void m4(Deep& c) { mk<P, M, 0>(c); mk<P, M, 1>(c); mk<P, M, 2>(c); mk<P, M, 3>(c); }
#define DOPS 4, 4, 4, 4, 4, 4, 4, 0, 0, 8, 8, 2, 2, 4, 0, 4
#define UORD 10, 11, 12, 13, 14, 15, 16
constexpr int dops_[16] = {DOPS}, uord_[17] = {0, 1, 2, 3, 4, 5, 6, 7, 8, 9, UORD};
constexpr int dops(int S) { return dops_[S]; }
constexpr int dcum(int S) { int n = 12; for (int k = 0; k <= S; ++k) n += dops_[k]; return n; }
constexpr int place(int U) { int k = 0; while (uord_[k] != U) ++k; return k; }
static_assert(dcum(15) == 68 && dcum(0) == 16 && dcum(2) == 24 && dcum(6) == 40, "the single operations keep their places");
constexpr int dyoung(int S, int U) { return dcum(S) - 4 * (place(U) + 1) + (S == 1 || S == 2); }
static_assert(dyoung(10, 10) >= 0 && dyoung(11, 11) >= 0 && dyoung(12, 12) >= 0 && dyoung(13, 13) >= 0 && place(13) < place(14), "");
#define SPOS 52
__device__ __forceinline__ void fetch_status(Deep& c) {
    if (c.m4 == 0u) {
        if (c.nq) dma16sc(c.lane16, c.ws, OFF_NB + 1024);
        else dma4sc(c.tad, c.ws, OFF_TAG + 512);
    }
    SB
}
template <int U, int n> __device__ __forceinline__ void landed(Deep& c) {
    constexpr int y = n - 4 * (place(U) + 1);
    if constexpr (n > SPOS && 4 * (place(U) + 1) <= SPOS) { WAITCNT(y + 1, 15); asm volatile("s_cmp_eq_u32 %0, 0\n\ts_cbranch_scc1 1f\n\ts_waitcnt vmcnt(%1)\n1:" :: "s"(c.m4), "n"(y) : "memory", "scc"); }
    else WAITCNT(y, 15);
    SB
}
#define FSK 0
template <int S, int I> __device__ __forceinline__ void dissue1(Deep& c) {
    constexpr int n = dcum(S - 1) + I, U = uord_[n >> 2];
    static_assert(S >= (U < 15 ? U - 3 : U == 15 ? 13 : 15), "slot in use");
    static_assert((SPOS & 3) == 0 && SPOS >= 40 && SPOS <= 60, "");
    if constexpr (n == SPOS) fetch_status(c);
    if constexpr (U < NP) {
        if constexpr ((n & 3) == 0 || (I == 0 && S <= 9)) { prepB<U>(c); asm volatile("s_nop 0"); }
        issueB<n & 3>(c);
    } else {
        if constexpr ((n & 3) == 0) { set_m0<FOFF[U - NP]>(c.ldsb); asm volatile("s_nop 0"); }
        constexpr unsigned long long rows = 0x00ff00ff00ff00ffull | 0xff00ull << 16 * (U - NP);
        if constexpr ((n & 1) && FSK)
            asm volatile("s_mov_b32 exec_lo, %3\n\ts_mov_b32 exec_hi, %4\n\tglobal_load_lds_dwordx4 %0, %1 offset:%2 sc1\n\ts_mov_b64 exec, -1"
                         :: "v"(c.lane16), "s"(c.wsf + (U - NP) * 16384), "n"((n & 3) * 1024), "n"((unsigned)rows), "n"((unsigned)(rows >> 32)) : "memory");
        else dma16sco<(n & 3) * 1024>(c.lane16, c.wsf + (U - NP) * 16384);
    }
    SB
}
template <int S, int I0, int I1> __device__ __forceinline__ void dissue(Deep& c) {
    if constexpr (I0 < I1 && I0 < dops(S)) { if constexpr (I0 >= 0) dissue1<S, I0>(c); dissue<S, I0 + 1, I1>(c); }
}
__device__ __forceinline__ unsigned tag_addr(int lane, int unit, int nq, int st, int xcd) {
    const unsigned l4 = (unsigned)lane * 4u;
    const unsigned a0 = (unsigned)(WS_FLAG + unit * 4) + l4 * 32u;
    const unsigned a1 = (((unsigned)(nq * 256) + l4 * 64u) & 768u) + (unsigned)(st * 4);
    const unsigned a2 = (unsigned)(WS_FLAG + xcd * 128 - 64) + l4;
    const unsigned lo = l4 < 32u ? a0 : a1, hi = l4 < 168u ? a2 : (unsigned)(st * 4);
    return l4 < 64u ? lo : hi;
}
__device__ __forceinline__ void publish(Deep& c, unsigned sv) {
    asm volatile("s_cmp_eq_u64 %3, 0\n\ts_cbranch_scc1 1f\n\ts_mov_b64 exec, %3\n\tglobal_store_dword %0, %1, %2 sc1\n\ts_mov_b64 exec, -1\n1:"
                 :: "v"(c.tad), "v"(sv), "s"(c.ws), "s"(c.pmask) : "memory", "scc");
}
__device__ __forceinline__ void produce(Deep& c) {
    if (c.m1 == 0u) {
        const int unit = c.unit;
        const unsigned p8 = (c.lane16 << 4) & 0x300u, e2 = (c.lane16 >> 5) & 0x1eu;
        const unsigned rb = (unsigned)(OFF_RAW + c.c * 32) + ((e2 << 2) & 16u) + ((e2 >> 1) & 12u) + (e2 & 2u);
        const unsigned dm = p8 == (c.lane16 & 0x300u) ? ~0u : 0u;
        const int m = unit < 24 ? 2 * (unit & 6) + (unit & 1) + ((unit >> 2) & 2) : (unit & 1) + 2;
        unsigned fo = (unsigned)(WS_FRAG + c.c * 65536 + (unit >> 4) * 1024 + m * 64) + e2 * 2048u + ((c.lane16 & 0x30u) ^ (unsigned)((m & 4) << 3));
        if (unit >= 24) fo = (fo + (c.lane16 & 0x300u)) ^ ((c.lane16 >> 3) & 0x20u);
#pragma unroll
        for (int pq = 0; pq < 2; ++pq) {
            u32x4 fr;
#pragma unroll
            for (int d = 0; d < 4; ++d) {
                const unsigned rp = rb + (d < 3 ? p8 : 0u) + (unsigned)(pq * (NF * 128) + 1024 * d);
                fr[d] = LDS(const uint16_t, rp) | ((unsigned)LDS(const uint16_t, rp + 128) << 16);
            }
            fr[3] &= dm;
            st16sc(fo + pq * 2048u, c.ws, fr);
        }
    }
    SB
}
__device__ __forceinline__ void check_flags(Deep& c) {
    const unsigned long long open = __builtin_amdgcn_ballot_w64(c.tag == c.pl) & (c.nq ? 0x7ffffff0000ull : 0x3ffffff0000ull);
    if (__builtin_expect(open != 0ull, 0)) {
#pragma unroll 1
        for (int it = 0; it < (1 << 22); ++it) {
            const unsigned v = __hip_atomic_load((__attribute__((address_space(1))) unsigned*)((gp)c.ws + c.tad), __ATOMIC_RELAXED, __HIP_MEMORY_SCOPE_AGENT);
            if ((__builtin_amdgcn_ballot_w64(v == c.tag) & open) == 0ull) break;
            __builtin_amdgcn_s_sleep(2);
        }
    }
    asm volatile("" ::: "memory");
    SB
}
constexpr int x3(int P) { return (P == P_FLAG || P == P_FLAG + 1) + (P == P_POLL || P == P_POLL + 1); }
#define TOP9 0
#define DTOPS 2, 2, 4, 2, 2, 2, 4, 2, 4, TOP9, 4, 4, 4
constexpr int dtop_[13] = {DTOPS};
constexpr int dtop(int P) { return dtop_[P]; }
template <int P, int N> __device__ __forceinline__ void dbody(Deep& c) {
    constexpr int top = dtop(P);
    m1<P - 1, N>(c);
    rd1<P, N>(c);
    if constexpr (N >= 12 && P < 8) dissue<P, top + N - 12, (N == 15 ? 16 : top + N - 11)>(c);
    if constexpr (N < 15) dbody<P, N + 1>(c);
}
template <int P, int N0, int N1> __device__ __forceinline__ void rdn(Deep& c) { if constexpr (N0 < N1) { rd1<P, N0>(c); rdn<P, N0 + 1, N1>(c); } }
#define GAP9 2
template <int P, int N> __device__ __forceinline__ void dnow(Deep& c) {
    m1<P, N>(c);
    if constexpr (P == 8) dissue<P, N - 16 + dops(P), N - 15 + dops(P)>(c);
    else if constexpr (N == 15) dissue<P, dtop(P) + N / GAP9, 16>(c);
    else if constexpr (N % GAP9 == GAP9 - 1) dissue<P, dtop(P) + N / GAP9, dtop(P) + N / GAP9 + 1>(c);
    if constexpr (N < 15) dnow<P, N + 1>(c);
}
#define WPOS 7
#define GAPL 3
constexpr int dbef(int S, int N) { const int k = (N + GAPL - 2) / GAPL; return N > 15 || k > dops(S) ? dops(S) : k; }
template <int S, int N> __device__ __forceinline__ void dgap(Deep& c) { dissue<S, dbef(S, N), dbef(S, N + 1)>(c); }
template <int P, int N> __device__ __forceinline__ void dlate(Deep& c) {
    m1<P - 1, N>(c);
    if constexpr (N < 4) rd1<P, (N ? N + 4 : 0)>(c);
    if constexpr (N >= WPOS && N < WPOS + 8) {
        if constexpr (N == WPOS) landed<P, dcum(P) + dbef(P + 1, N)>(c);
        rd1<P, (N < WPOS + 4 ? N - WPOS + 1 : N - WPOS + 4)>(c);
    }
    dgap<P + 1, N>(c);
    if constexpr (N < 15) dlate<P, N + 1>(c);
}
__device__ __forceinline__ void getw2(Deep& c) {
#pragma unroll
    for (int k = 0; k < 4; ++k) {
        const u32x2 v = LDS(const u32x2, (unsigned)(OFF_WD2 + k * 32) + ((c.lane16 >> 5) & 24u));
        c.w2[4 * k] = lo_f(v[0]); c.w2[4 * k + 1] = hi_f(v[0]); c.w2[4 * k + 2] = lo_f(v[1]); c.w2[4 * k + 3] = hi_f(v[1]);
    }
#pragma unroll
    for (int k = 0; k < 16; ++k) { if (IW) c.w2[k] = __builtin_amdgcn_fmed3f(__builtin_amdgcn_rcpf(c.w2[k]), -0x1p127f, 0x1p127f); PIN(c.w2[k]); }
        SB
}
template <int P> struct DStep {
    static __device__ __forceinline__ void run(Deep& c) {
        TW(0, c.ts[P])
        SB
        if constexpr (P == P_FLAG) publish(c, c.tag + 1u);
        if constexpr (P == 1) { dma4((unsigned)(c.nq * 128) + ((c.lane16 >> 2) & 124u), c.wd2, OFF_WD2); SB }
        if constexpr (P == 4 && W2P == 3) getw2(c);
        if constexpr (P < 8) dissue<P, 0, dtop(P)>(c);
        dbody<P, 0>(c);
        if constexpr (P == W2P && P >= 4 && P < 8) getw2(c);
        if constexpr (P == 8) { WAITCNT(63, 0); BARRIER; TX(1) dnow<8, 0>(c); if constexpr (W2P == 8) getw2(c); TX(2) WAITX(dyoung(8, 9), 1, c.m3) rdn<9, 1, 5>(c); rdn<9, 8, 12>(c); }
        if constexpr (P == P_POLL) { if (c.m3 == 0u) dma4sc(c.tad, c.ws, OFF_TAG + 256); }
        SB
        if constexpr (P == SPD) SPOT(2)
        TW(1, c.ts[P])
        if constexpr (P + 1 < P_FLAG && P < 2 - PEAR) WAITX(dyoung(P, P + 1), 2, c.m1)
        else if constexpr (x3(P) > 0) WAITX(dyoung(P, P + 1), x3(P), c.m3)
        else WAITCNT(dyoung(P, P + 1), P == 8 ? 15 : 0);
        TW(2, c.ts[P])
        BARRIER;
        if constexpr (P < 8) DStep<P + 1>::run(c);
    }
};
static_assert(P_CHK == 9 && P_STAT == 9, "");
__device__ __forceinline__ void dlast(Deep& c) {
    TS(c.ts[9])
    rd1<9, 0>(c); rdn<9, 5, 8>(c);
    c.pl = LDSV(OFF_TAG + 256 + (c.lane16 >> 2)); SB
    dissue<9, 0, dtop(9)>(c);
    TX(3)
    dnow<9, 0>(c);
    TX(4)
    check_flags(c);
    dissue<10, 0, 16>(c);
    TX(5)
    landed<10, dcum(10)>(c);
    rdn<10, 1, 5>(c); rdn<10, 8, 12>(c);
    TS(c.ts[10])
    BARRIER;
    TS(c.ts[11])
    rd1<10, 0>(c); rdn<10, 5, 8>(c);
    dissue<11, 0, 16>(c);
    dlate<11, 0>(c);
    TS(c.ts[12])
    dlate<12, 0>(c);
    TS(c.ts[13])
}

struct Cross : Com {
    const char* tb[4];
    unsigned ga[40];
    unsigned re[2], rx, rx0[2], six, wf[2];
    s16x4 wa[2];
    bf16x8 wb[2], wr[2];
    u32x4 fe[2][2][2];
    f32x4 ew[NF], zs[4];
    u32x4 X[4], Y[NP], bp[4];
    float k0, k1, k2;
    u32x4 on[4], s1, zq;
};
template <int I> __device__ __forceinline__ void issueA(Cross& c, unsigned off) { dma16o<I * 1024>(off, c.tb[I]); }
template <int K, int S, int N> __device__ __forceinline__ void sw1(Cross& c) {
    constexpr int a = 1 + 4 * K + (S == 1 ? 2 : (S == 3 ? 1 : 0)), b = 1 + 4 * K + (S == 0 ? 1 : (S == 2 ? 2 : 3)), Q = N >> 2, R = N & 3;
    float x = c.ew[2 * a + Q][R], y = c.ew[2 * b + Q][R];
    if (S >= 2) asm volatile("v_permlane32_swap_b32 %0, %1" : "+v"(x), "+v"(y)); else asm volatile("v_permlane16_swap_b32 %0, %1" : "+v"(x), "+v"(y));
    c.ew[2 * a + Q][R] = x; c.ew[2 * b + Q][R] = y;
}
template <int K, int S, int N0 = 0, int N1 = 8> __device__ __forceinline__ void swn(Cross& c) { if constexpr (N0 < N1) { sw1<K, S, N0>(c); swn<K, S, N0 + 1, N1>(c); } }
constexpr int sw0(int P) { return P <= 3 ? 0 : P <= 5 ? 8 : P == 6 ? 20 : P == 7 ? 32 : 40; }
template <int P, int N0, int N1> __device__ __forceinline__ void tsw(Cross& c) {
    if constexpr (N0 < N1 && sw0(P) + N0 < sw0(P + 1)) { sw1<((sw0(P) + N0) >> 5), ((sw0(P) + N0) >> 3) & 3, (sw0(P) + N0) & 7>(c); tsw<P, N0 + 1, N1>(c); }
}
constexpr int fbuf(int P) { return P & 1; }
template <int P, int N> __device__ __forceinline__ void cmm(Cross& c) {
    if constexpr (P >= 0) {
        constexpr int B = fbuf(P);
        if constexpr (N < 2)
            asm volatile("v_smfmac_f32_16x16x64_bf16 %0, %1, %2, %3" : "+v"(c.zs[P ? (P - 1) >> 2 : 3])
                         : "v"(c.on[P ? (P - 1) & 3 : 0]), "v"(__builtin_shufflevector(c.fe[B][N][0], c.fe[B][N][1], 0, 1, 2, 3, 4, 5, 6, 7)), "v"(c.six));
        else if constexpr (N < 4) c.ew[2 * P + (N & 1)] = mf0(P ? c.wb[0] : c.wr[0], c.fe[B][0][N & 1]);
        else mfa(c.ew[2 * P + (N & 1)], P ? c.wb[1] : c.wr[1], c.fe[B][1][N & 1]);
    }
    SB
}
template <int P, int N> __device__ __forceinline__ void rdE(Cross& c) {
    c.fe[fbuf(P)][N >> 1][N & 1] = LDS(const u32x4, c.re[N >> 1] + (P & 3) * SLOT + (N & 1) * 2048);
    SB
}
template <int K> __device__ __forceinline__ void zpack(Cross& c) {
    const float a = add_o(c.zs[K][0], c.zs[K][1]), b = add_o(c.zs[K][2], c.zs[K][3]);
    const unsigned r = cvt_o(a, b);
    c.zq[K] = r;
    SB
}
template <int K> __device__ __forceinline__ void zclear(Cross& c) { c.zs[K] = f32x4{0.f, 0.f, 0.f, 0.f}; PIN(c.zs[K]); SB }
template <int P> __device__ __forceinline__ void rdX(Cross& c) {
    if constexpr (P == 0) c.X[0] = u32x4{LDS(const unsigned, c.rx0[0]), LDS(const unsigned, c.rx0[0] + 2048), LDS(const unsigned, c.rx0[1]), LDS(const unsigned, c.rx0[1] + 2048)};
    else if constexpr ((P & 3) == 2)
        asm volatile("s_mov_b64 exec, %5\n\tds_read_b128 %0, %4 offset:16384\n\tds_read_b128 %1, %4 offset:18432\n\tds_read_b128 %2, %4 offset:32768\n\tds_read_b128 %3, %4 offset:34816\n\ts_mov_b64 exec, -1"
                     : "=&v"(c.X[0]), "=&v"(c.X[1]), "=&v"(c.X[2]), "=&v"(c.X[3]) : "v"(c.rx), "s"(0x0000ffff0000ffffull) : "memory");
    else if constexpr ((P & 3) == 0)
        asm volatile("s_mov_b64 exec, %5\n\tds_read_b128 %0, %4 offset:49152\n\tds_read_b128 %1, %4 offset:51200\n\tds_read_b128 %2, %4\n\tds_read_b128 %3, %4 offset:2048\n\ts_mov_b64 exec, -1"
                     : "+v"(c.X[0]), "+v"(c.X[1]), "+v"(c.X[2]), "+v"(c.X[3]) : "v"(c.rx), "s"(0xffff0000ffff0000ull) : "memory");
    SB
}
__device__ __forceinline__ unsigned perm_o(unsigned hi, unsigned lo, unsigned s) { unsigned r; asm volatile("v_perm_b32 %0, %1, %2, %3" : "=v"(r) : "v"(hi), "v"(lo), "s"(s)); return r; }
__device__ __forceinline__ u32x4 pairs(unsigned a, unsigned b, unsigned c, unsigned d) {
    return u32x4{perm_o(b, a, 0x05040100u), perm_o(b, a, 0x07060302u), perm_o(d, c, 0x05040100u), perm_o(d, c, 0x07060302u)};
}
template <int K, int Q0 = 0, int Q1 = 4> __device__ __forceinline__ void mkY(Cross& c) {
#pragma unroll
    for (int q = Q0; q < Q1; ++q) c.Y[4 * K + q] = pairs(c.X[0][q], c.X[1][q], c.X[2][q], c.X[3][q]);
    SB
}
#define Y1P 4
#define Y2P 0
#define B8 1
#define G8 1
#define CSP 4
__device__ __forceinline__ void cset(Cross& c) {
    const unsigned r6 = c.lane16 * 4u + (unsigned)c.rho * 256u;
    const unsigned am = (c.lane16 >> 8) + (unsigned)c.rho;
    c.wf[0] = (r6 & 0x3c0u) | (((am << 4) ^ ((r6 >> 3) & 0x20u)) & 0x30u) | SLOT;
    c.wf[1] = c.wf[0] + OFF_B;
    const unsigned wap = OFF_SW2 + (c.lane16 & 0x3c0u) + ((c.lane16 >> 1) & 0x18u);
    c.wa[0] = tr_read(wap); c.wa[1] = tr_read(wap + 32);
    const unsigned r2 = (c.lane16 >> 3) & 30u, s1 = OFF_SW1 + min(r2, 24u), sg = s1 + (am & 3u) * 52u;
#pragma unroll
    for (int k = 0; k < 4; ++k) {
        const unsigned a = (k < 3 ? sg : s1) + k * 208;
        const unsigned v = LDS(const uint16_t, a) | ((unsigned)LDS(const uint16_t, a + 26) << 16);
        c.s1[k] = r2 < 26u ? v : 0u;
    }
    PIN(c.s1);
    PIN(c.wf[0]); PIN(c.wf[1]); PIN(c.wa[0]); PIN(c.wa[1]);
        SB
}
#define B0P 0
__device__ __forceinline__ void bp0(Cross& c) {
#pragma unroll
    for (int r = 0; r < 4; ++r) { c.bp[r][3] = cvt_o(c.ew[0][r], c.ew[1][r]); c.bp[r][0] = cvt_o(c.ew[2][r], c.ew[3][r]); }
    c.k0 = -2.0f * 0.7978845608028654f * 1.4426950408889634f / 64.0f; c.k1 = c.k0 * (0.044715f / 4096.0f); c.k2 = -64.0f / 1.4426950408889634f;
    PIN(c.k0); PIN(c.k1); PIN(c.k2);
    SB
}
template <int P> struct CStep {
    static __device__ __forceinline__ void run(Cross& c) {
        TW(0, c.ts[P])
        SB
        cmm<P - 1, 2>(c); rdE<P, 0>(c);
        cmm<P - 1, 3>(c); rdE<P, 1>(c);
        cmm<P - 1, 4>(c); rdE<P, 2>(c);
        cmm<P - 1, 5>(c); rdE<P, 3>(c);
        cmm<P - 1, 0>(c);
        if constexpr (P + 3 < NP) set_m0<((P + 3) & 3) * SLOT>(c.ldsb);
        cmm<P - 1, 1>(c);
        rdX<P>(c);
        constexpr bool xw = P && !(P & 1);
#define GA(J) if constexpr (P + 3 < NP) issueA<J>(c, c.ga[4 * P + (J)]); SB
        if constexpr (P == CSP && P) cset(c);
        if constexpr (P == 4 || P == 8) zclear<P / 4>(c);
        if constexpr (P == 2 || P == 6) zpack<(P == 2 ? 3 : 0)>(c);
        if constexpr (P == 8) {
            WAITCNT(63, 0); SB
            if constexpr (B8 == 0) BARRIER;
            cmm<8, 2>(c); cmm<8, 3>(c); GA(0) cmm<8, 4>(c); cmm<8, 5>(c); GA(1) cmm<8, 0>(c); cmm<8, 1>(c);
            if constexpr (B0P == 0) bp0(c);
            if constexpr (Y1P == 0) mkY<1>(c);
            SB
            if constexpr (B8 == 1) BARRIER;
            if constexpr (B0P && Y1P) asm volatile("s_nop 3");
#define GB(J) issueA<J>(c, c.ga[36 + (J)]); SB
            if constexpr (G8 == 0) {
                swn<1, 1>(c); zpack<1>(c); swn<1, 2>(c); swn<1, 3>(c);
#pragma unroll
                for (int r = 0; r < 4; ++r) c.bp[r][1] = cvt_o(c.ew[10][r], c.ew[11][r]);
                SB
                GA(2) GA(3) set_m0<0>(c.ldsb); asm volatile("s_nop 0"); GB(0) GB(1) GB(2) GB(3)
            } else {
                swn<1, 1>(c); zpack<1>(c); GA(2) swn<1, 2, 0, 4>(c); GA(3) set_m0<0>(c.ldsb); swn<1, 2, 4, 8>(c); GB(0) swn<1, 3, 0, 4>(c); GB(1) swn<1, 3, 4, 8>(c); GB(2)
#pragma unroll
                for (int r = 0; r < 4; ++r) c.bp[r][1] = cvt_o(c.ew[10][r], c.ew[11][r]);
                SB
                if constexpr (Y1P == 1) mkY<1>(c);
                GB(3)
                if constexpr (Y1P == 2) mkY<1>(c);
            }
        }
        else if constexpr (xw) { tsw<P, 0, 4>(c); WAITCNT(63, 0); GA(0) tsw<P, 4, 6>(c); GA(1) tsw<P, 6, 8>(c); GA(2) tsw<P, 8, 10>(c); GA(3) tsw<P, 10, 12>(c); }
        else { GA(0) GA(1) tsw<P, 0, 4>(c); GA(2) tsw<P, 4, 8>(c); GA(3) tsw<P, 8, 12>(c); }
        if constexpr ((P == 3 && CSP == 0) || P + 10 == CSP) cset(c);
        if constexpr (P == 0) { c.Y[12] = pairs(c.X[0][0], c.X[0][1], c.X[0][2], c.X[0][3]); SB }
        if constexpr (P == 4) mkY<0>(c);
        SB
        TW(1, c.ts[P])
        if constexpr (P == 8) { TX(0) }
        WAITCNT(P == 8 ? 12 : 8, 0);
        TW(2, c.ts[P])
        BARRIER;
        if constexpr (P < 8) CStep<P + 1>::run(c);
    }
};

#define PFC 144
#define KOFF 0x700
extern "C" __device__ __attribute__((used, noinline, aligned(4096))) void k_front() { asm volatile(".fill %0, 4, 0xbf800000" :: "n"((KOFF - 128) / 4)); }
#define WPRE 6
__global__ __launch_bounds__(512) void k_main(const int* __restrict__ idx, const uint16_t* __restrict__ table,
        const uint16_t* __restrict__ w_bi, const uint16_t* __restrict__ w_deep1, const uint16_t* __restrict__ w_cross,
        unsigned* __restrict__ ws32, const uint16_t* __restrict__ se_w1, const uint16_t* __restrict__ se_w2,
        const uint16_t* __restrict__ w_deep2_, __hip_bfloat16* __restrict__ out_, unsigned long long* __restrict__ stamps) {
    SB
    const int tid = threadIdx.x, lane = tid & 63;
    const int w = __builtin_amdgcn_readfirstlane(tid >> 6);
    const int bid = blockIdx.x;
    const int xcd = bid & 7, slot = bid >> 3;
    const int st = xcd * 8 + (slot >> 2), nq = slot & 3;
    const int s0 = st * 64;
    const int rho = (st + nq) & 3, cw = w & 3;
    const int l15 = lane & 15, g = lane >> 4;
    LDS(u32x2, OFF_FL + tid * 8) = u32x2{0u, 0u};
    unsigned long long pc;
    {
        const unsigned ln = min((unsigned)(4 + w * 4 + (lane & 3) + ((lane >> 2) & 1) * (32 + nq * 32)), (unsigned)PFC) * 128u;
        asm volatile("s_getpc_b64 %0\n\ts_mov_b32 m0, %2\n\ts_mov_b64 exec, 0xff\n\tglobal_load_lds_dword %1, %0\n\ts_mov_b64 exec, -1"
                     : "=&s"(pc) : "v"(ln), "n"(OFF_JNK) : "memory");
    }
    SB
    float gc0 = -2.0f * 0.7978845608028654f * 1.4426950408889634f, gc1 = gc0 * 0.044715f;
#define FIN_ST(c)
#define FIN_SA(c)
#define FINISH(c) STAMP(5) FIN_ST(c) FIN_SA(c)
    unsigned lane16 = (unsigned)lane * 16u;
    int deep = w >> 2;
    Deep d;
    d.ldsb = (unsigned)(cw * 4096);
    unsigned o2 = min(lane16, 624u), im0 = (unsigned)(OFF_IDX + cw * 2048), io = (unsigned)((s0 + 16 * cw) * (NF * 4));
    mkboff(d, lane, cw, nq);
#pragma unroll
    for (int k = 0; k < 4; ++k) { d.sG[k] = (unsigned)(((k + rho) & 3) * (2 * 32768)); PINS(d.sG[k]); }
    d.unit = xcd + 8 * ((st & 7) >> 1);
    d.m1 = (unsigned)((nq ^ 1) | (st & 1) | ((d.unit + 32 - NF) >> 5));
    PIN(lane16); PIN(o2); PINS(im0); PINS(io); PINS(deep); PINS(d.ldsb); PINS(d.m1);
    asm volatile("s_mov_b32 m0, %0" :: "s"(im0) : "memory");
    SB
    asm volatile("" :: "s"(idx), "s"(table), "s"(se_w1), "s"(se_w2), "s"(w_bi), "s"(w_deep1), "s"(w_cross), "s"(ws32));
    asm volatile("s_cmp_lg_u32 %3, 0\n\ts_cbranch_scc1 1f\n\tglobal_load_lds_dwordx4 %0, %2\n\tglobal_load_lds_dwordx4 %1, %2 offset:1024\n1:"
                 :: "v"(lane16), "v"(o2), "s"((const char*)idx + io), "s"(deep) : "memory", "scc");
    SB
    typedef const __attribute__((address_space(4))) unsigned long long* kap;
    kap ka = (kap)__builtin_amdgcn_kernarg_segment_ptr();
    PINS(ka);
    d.wd2 = (const char*)ka[8];
    const gp out = (gp)ka[9];
    unsigned long long w1p;
    asm volatile("s_mov_b64 %0, %1" : "=s"(w1p) : "s"(w_deep1));
    d.rw1 = __builtin_amdgcn_make_buffer_rsrc((void*)w1p, (short)0, NF * 32768, 0x00020000);
    if (deep) {
        unsigned to = min(lane16, 80u) * 8u + (unsigned)((bid * 4 + cw) * 768);
        if (T3N && T3P == 0) to = lane16 < 128u ? to : (lane16 & ((T3N - 1) * 16u)) * (32u * T3S) + (unsigned)((T30 + (slot * 4 + cw) * T3N * T3S) * 512 + 384);
        asm volatile("s_mov_b32 m0, %2\n\ts_nop 0\n\tbuffer_load_dword %0, %1, 0 offen lds" :: "v"(to), "s"(d.rw1), "n"(OFF_JNK) : "memory");
    }
    prepB<0>(d);
    TS(tz1)
    BARRIER;
    TS(tz2)
    Cross x;
    x.c = cw; x.nq = nq; x.rho = rho; x.st = st; x.lane16 = lane16; x.ws = (char*)ws32; x.ldsb = d.ldsb;
    unsigned srcA, srcB, ib[4];
    u32x2 ia[NP], ic[NP];
    if (deep) {
        issueB<0>(d); issueB<1>(d); issueB<2>(d); issueB<3>(d);
        SB
        const unsigned wo = (unsigned)(cw * 2048 + (lane >> 3) * 128 + nq * 32 + (lane & 1) * 16 + (lane & 6) * 2);
        dma4(wo, w_bi, OFF_WBI + cw * 512); dma4(wo + 1024u, w_bi, OFF_WBI + cw * 512 + 256);
        dma4((unsigned)min(cw * 64 + lane, 168) * 4u, se_w1, OFF_SW1 + cw * 256);
        const int k = (l15 & 1) | ((l15 >> 2) & 2), r = 4 * cw + g;
        dma4((unsigned)((r < 13 ? r : 12) * (NF * 2) + k * 16 + (k == 3 ? 0 : ((l15 + 2 * rho) & 6) * 2)), se_w2, OFF_SW2 + cw * 256);
        SB
        if (__builtin_expect(d.m1 == 0u, 1)) {
            const unsigned r0 = (unsigned)(16 * cw + (lane >> 3)), lo = (lane16 & 0x70u) + (unsigned)(d.unit * 128);
            dma16(min(r0, 2u * NF - 1) * (NF * 128) + lo, w_cross, OFF_RAW + cw * 2048);
            dma16(min(r0 + 8u, 2u * NF - 1) * (NF * 128) + lo, w_cross, OFF_RAW + cw * 2048 + 1024);
            dma4sc((lane16 >> 2) + (unsigned)(cw * 256), ws32, OFF_NB + cw * 256);
        }
        if (cw == 3) dma4sc(tag_addr(lane, d.unit, nq, st, xcd), ws32, OFF_TAG);
        if ((cw ^ 2) + nq == 0 && lane < 32) st16sc(lane16, (char*)ws32 + WS_SUM + st * 512, u32x4{0u, 0u, 0u, 0u});
        SB
        if (T3N && T3P == 1)
            asm volatile("s_mov_b32 m0, %2\n\ts_nop 0\n\tbuffer_load_dword %0, %1, 0 offen lds" :: "v"((lane16 & ((T3N - 1) * 16u)) * (32u * T3S) + (unsigned)((T30 + (slot * 4 + cw) * T3N * T3S) * 512 + 384)), "s"(d.rw1), "n"(OFF_JNK) : "memory");
        if ((C3E || nq != 3) && (!PB1 || d.m1 != 0u)) { prepB<1>(d); asm volatile("s_nop 0"); issueB<0>(d); issueB<1>(d); issueB<2>(d); issueB<3>(d); }
        SB
    } else {
        Cross& c = x;
        srcA = (unsigned)(((lane & 7) ^ g) * 16); srcB = srcA ^ 64u;
        unsigned ib0 = (unsigned)(OFF_IDX + cw * 2048 + (lane >> 3) * 104);
#pragma unroll
        for (int k = 0; k < 4; ++k) { ib[k] = ib0 + (unsigned)(((k + rho) & 3) * 8); PIN(ib[k]); c.tb[k] = (const char*)table - k * 1024; PINS(c.tb[k]); }
        PIN(srcA); PIN(srcB); PIN(ib0);
        set_m0<0>(c.ldsb);
        asm volatile("s_branch .Lpb1\n" PADTO(1));
        SB
        WAITCNT(0, 15);
        asm volatile("" ::: "memory");
        STAMP(1)
        TP(c.tq[0])
        ia[0] = LDS(const u32x2, ib0 + 96); ic[0] = LDS(const u32x2, ib0 + 96 + 832);
#pragma unroll
        for (int q = 1; q < 3; ++q) { ia[q] = LDS(const u32x2, ib[q - 1]); ic[q] = LDS(const u32x2, ib[q - 1] + 832); }
        SB
#define GATHER(q) issueA<0>(c, ia[q][0] * 128u + srcA); issueA<1>(c, ic[q][0] * 128u + srcB); issueA<2>(c, ia[q][1] * 128u + srcA); issueA<3>(c, ic[q][1] * 128u + srcB);
        GATHER(0)
        set_m0<SLOT>(c.ldsb);
        SB
        GATHER(1)
        set_m0<2 * SLOT>(c.ldsb);
        TP(c.tq[1])
    }
    TS(tz3)
    int crs = deep ^ 1;
    PINS(crs); PINS(deep);
    if (crs == 0) {
        Deep& c = d;
        int lane;
        asm volatile("v_mbcnt_lo_u32_b32 %0, -1, 0\n\tv_mbcnt_hi_u32_b32 %0, -1, %0" : "=v"(lane));
        const int l15 = lane & 15, g = lane >> 4, tid = w * 64 + lane;
        c.c = cw; c.nq = nq; c.rho = rho; c.st = st; c.lane16 = (unsigned)lane * 16u; c.ws = (char*)ws32;
        c.m3 = (unsigned)(cw ^ 3); c.m4 = c.m3 | min(c.m1, (unsigned)nq);
        c.tad = tag_addr(lane, c.unit, nq, st, xcd);
        PIN(c.tad);
        SB
        mkboff(d, lane, cw, nq);
        {
            const unsigned a0 = (unsigned)(cw * 4096 + (cw & 1) * 2048 + l15 * 128 + ((4 * (cw >> 1) + g) ^ (l15 >> 1)) * 16);
            const int q = l15 >> 2, p = l15 & 3;
            const unsigned b0 = (unsigned)(OFF_B + cw * 4096 + (8 * g + q) * 128 + ((p >> 1) ^ (q & 2) ^ ((g & 1) << 2)) * 16 + (p & 1) * 8);
#pragma unroll
            for (int k = 0; k < 4; ++k) { c.ra[k] = a0 ^ (unsigned)(k * 4096); c.rb[k] = b0 ^ (unsigned)(k * 32); PIN(c.ra[k]); PIN(c.rb[k]); }
#pragma unroll
            for (int k = 0; k < 16; ++k) { c.ac[k >> 2][k & 3] = f32x4{0.f, 0.f, 0.f, 0.f}; PIN(c.ac[k >> 2][k & 3]); }
        }
        unsigned exw[4], fl = (c.lane16 >> 2) + (unsigned)(OFF_FL + cw * 256), fr = c.lane16 + OFF_FL, s2 = c.lane16 >> 3, s8 = c.lane16 >> 1;
#pragma unroll
        for (int k = 0; k < 4; ++k) { exw[k] = c.lane16 + (unsigned)(OFF_B + (cw ^ k) * 4096); PIN(exw[k]); }
        double big = 0x1.8p52;
        float va[2] = {0.f, 0.f};
        const unsigned dl = (unsigned)(l15 - 4 * g), on = (dl & 1u) ? 0x3f800000u : 0x3f80u;
        u32x4 unit = {dl < 2u ? on : 0u, dl - 2u < 2u ? on : 0u, dl < 2u ? on : 0u, dl - 2u < 2u ? on : 0u};
        PIN(fl); PIN(fr); PIN(s2); PIN(s8); PIN(big); PIN(gc0); PIN(gc1); PIN(va[0]); PIN(va[1]); PIN(unit);
        c.wsf = c.ws + WS_FRAG + nq * 65536 + cw * 4096;
        c.pmask = cw != 3 ? 0ull : (c.m1 == 0u ? 0x1ffull : 0x100ull);
        PINS(c.pmask); PINS(c.m3); PINS(c.m4); PINS(c.wsf);
        if ((!C3E && nq == 3) || (PB1 && c.m1 == 0u)) { prepB<1>(c); asm volatile("s_nop 0"); issueB<0>(c); issueB<1>(c); issueB<2>(c); issueB<3>(c); }
        TP(c.tq[0])
        WAITCNT(4, 0); BARRIER;
        TP(c.tq[1])
        if (PEAR) produce(c);
        prepB<2>(c); asm volatile("s_nop 0"); issueB<0>(c); issueB<1>(c); issueB<2>(c); issueB<3>(c);
        TP(c.tq[2])
        if (!PEAR) produce(c);
        c.tag = LDSV(OFF_TAG + (c.lane16 >> 2));
        PIN(c.tag);
        TP(c.tq[3])
        SCOUT(2)
        BARRIER;
        TP(c.tq[4])
        gp sums = (gp)c.ws + (unsigned)(WS_SUM + (s0 + 16 * cw) * 8), outp = out + (unsigned)((s0 + 16 * cw) * 2);
        PINS(sums); PINS(outp);
        DStep<0>::run(c);
        dlast(c);
        u32x4 px[3][2];
#define EXC(e) { _Pragma("unroll") for (int k = 0; k < 8; ++k) px[(e) - 1][k >> 2][k & 3] = cvt_o(c.ac[e][k >> 1][2 * (k & 1)], c.ac[e][k >> 1][2 * (k & 1) + 1]); SB }
#define EXA(e, k) (((e) == 3 ? 3 * SLOT : ((e) - 1) * 2048) + (k) * 1024)
#define EXW(e) { LDS(u32x4, exw[0] + EXA(e, 0)) = px[(e) - 1][0]; LDS(u32x4, exw[0] + EXA(e, 1)) = px[(e) - 1][1]; SB }
#define EX2(e, k) { px[(e) - 1][(k) >> 1][2 * ((k) & 1)] = cvt_o(c.ac[e][k][0], c.ac[e][k][1]); px[(e) - 1][(k) >> 1][2 * ((k) & 1) + 1] = cvt_o(c.ac[e][k][2], c.ac[e][k][3]); SB }
#define FBAR(n, N) if ((n) == FBP) { TE(c.tq[0]) landed<13, dcum(13) + dbef(14, N)>(c); TX(6) BARRIER; SB PRI(1) }
#define MG(m, k, N) mk<12, m, k>(c); dgap<14, N>(c);
        FBAR(0, 0)
        MG(3, 0, 0) MG(3, 1, 1) FBAR(4, 2) MG(3, 2, 2) MG(3, 3, 3) FBAR(5, 4) MG(2, 0, 4) MG(2, 1, 5) FBAR(6, 6) MG(2, 2, 6) MG(2, 3, 7)
        FBAR(1, 8)
#define FLP 7
#define FLG(n) if ((n) == FLP) { WAITCNT(0, 15); SB LDS(unsigned, fl) = fl; SB }
        FLG(0) MG(1, 0, 8) MG(1, 1, 9) FBAR(7, 10) FLG(1) EX2(3, 0) MG(1, 2, 10) FLG(2) EX2(3, 1) MG(1, 3, 11) FLG(3) EX2(3, 2)
        MG(0, 0, 12) FLG(4) EX2(3, 3) MG(0, 1, 13) FLG(5) EX2(2, 0) MG(0, 2, 14) FLG(6) EX2(2, 1) MG(0, 3, 15) FLG(7) EX2(2, 2)
        FBAR(2, 16)
        EX2(2, 3)
        FLG(8)
        if (!WORD) EXC(1)
        FBAR(3, 16)
        PRI(2)
        FLG(9)
        EXW(3) EXW(2)
        if (WORD) EXC(1)
        EXW(1)
        TE(c.tq[1])
        SB
        PRI(9)
        LDS(unsigned, fl + 1024) = fl;
        SB
        dissue<15, 0, 16>(c);
        publish(c, c.tag + 2u);
        unsigned long long watch = 0ull;
        if (c.m4 == 0u) {
            if (c.nq) {
                const u32x4 e4 = LDS(const u32x4, OFF_NB + c.lane16), l4 = LDS(const u32x4, OFF_NB + 1024 + c.lane16);
                watch = __builtin_amdgcn_ballot_w64(e4[0] == l4[0] || e4[1] == l4[1] || e4[2] == l4[2] || e4[3] == l4[3]);
            } else watch = __builtin_amdgcn_ballot_w64(LDSV(OFF_TAG + 512 + (c.lane16 >> 2)) == c.tag) & 0xe00ull;
        }
        SB
        TE(c.tq[2])
#pragma unroll 1
        for (int it = 0; it < (1 << 20) && __builtin_amdgcn_ballot_w64(LDSV(fr + 1024) == 0u) != 0ull; ++it) { }
        asm volatile("" ::: "memory");
        TE(c.tq[3])
        PRI(3)
        u32x2 pa[4], pb[4];
#pragma unroll
        for (int k = 0; k < 2; ++k) px[2][k] = LDS(const u32x4, exw[3] + EXA(3, k));
#pragma unroll
        for (int k = 0; k < 4; ++k) {
            pa[k] = *(volatile const __attribute__((address_space(3))) u32x2*)(size_t)(exw[1] + EXA(1, k >> 1) + (k & 1) * 8);
            pb[k] = *(volatile const __attribute__((address_space(3))) u32x2*)(size_t)(exw[2] + EXA(2, k >> 1) + (k & 1) * 8);
        }
        SB
#pragma unroll
        for (int k = 0; k < 4; ++k)
            asm volatile("v_mfma_f32_16x16x16_bf16 %0, %1, %2, %0" : "+v"(c.ac[0][k]) : "v"(u32x2{unit[0], unit[1]}), "v"(u32x2{px[2][k >> 1][2 * (k & 1)], px[2][k >> 1][2 * (k & 1) + 1]}));
        asm volatile("s_nop 2");
#pragma unroll
        for (int k = 0; k < 4; ++k) mfa(c.ac[0][k], unit, u32x4{pa[k][0], pa[k][1], pb[k][0], pb[k][1]});
        asm volatile("s_nop 4");
#pragma unroll
        for (int k = 0; k < 4; ++k) {
            PRI(4 + k)
#define LAST(n) if ((n) == LK) { WAITX(0, 1, c.m3) LDS(unsigned, fl + 2048) = fl; SB TX(0) }
            LAST(2 * k)
            float x[4] = {c.ac[0][k][0], c.ac[0][k][1], c.ac[0][k][2], c.ac[0][k][3]}, y[4];
            asm volatile("v_mul_f32 %0, %4, %4\n\tv_mul_f32 %1, %5, %5\n\tv_mul_f32 %2, %6, %6\n\tv_mul_f32 %3, %7, %7\n\t"
                         "v_fma_f32 %0, %0, %9, %8\n\tv_fma_f32 %1, %1, %9, %8\n\tv_fma_f32 %2, %2, %9, %8\n\tv_fma_f32 %3, %3, %9, %8\n\t"
                         "v_mul_f32 %0, %0, %4\n\tv_mul_f32 %1, %1, %5\n\tv_mul_f32 %2, %2, %6\n\tv_mul_f32 %3, %3, %7\n\t"
                         "v_exp_f32 %0, %0\n\tv_exp_f32 %1, %1\n\tv_exp_f32 %2, %2\n\tv_exp_f32 %3, %3"
                         : "=&v"(y[0]), "=&v"(y[1]), "=&v"(y[2]), "=&v"(y[3]) : "v"(x[0]), "v"(x[1]), "v"(x[2]), "v"(x[3]), "v"(gc0), "v"(gc1));
            LAST(2 * k + 1)
            if (!IW) {
            asm volatile("v_mul_f32 %0, %0, %10\n\tv_mul_f32 %1, %1, %11\n\tv_mul_f32 %2, %2, %12\n\tv_mul_f32 %3, %3, %13\n\t"
                         "v_add_f32 %4, 1.0, %4\n\tv_add_f32 %5, 1.0, %5\n\tv_add_f32 %6, 1.0, %6\n\tv_add_f32 %7, 1.0, %7\n\t"
                         "v_rcp_f32 %4, %4\n\tv_rcp_f32 %5, %5\n\tv_rcp_f32 %6, %6\n\tv_rcp_f32 %7, %7\n\t"
                         "v_fmac_f32 %8, %0, %4\n\tv_fmac_f32 %9, %1, %5\n\tv_fmac_f32 %8, %2, %6\n\tv_fmac_f32 %9, %3, %7"
                         : "+v"(x[0]), "+v"(x[1]), "+v"(x[2]), "+v"(x[3]), "+v"(y[0]), "+v"(y[1]), "+v"(y[2]), "+v"(y[3]), "+v"(va[0]), "+v"(va[1])
                         : "v"(c.w2[4 * k]), "v"(c.w2[4 * k + 1]), "v"(c.w2[4 * k + 2]), "v"(c.w2[4 * k + 3]));
            } else
            asm volatile("v_fma_f32 %0, %0, %10, %10\n\tv_fma_f32 %1, %1, %11, %11\n\tv_fma_f32 %2, %2, %12, %12\n\tv_fma_f32 %3, %3, %13, %13\n\t"
                         "v_rcp_f32 %0, %0\n\tv_rcp_f32 %1, %1\n\tv_rcp_f32 %2, %2\n\tv_rcp_f32 %3, %3\n\t"
                         "v_fmac_f32 %4, %6, %0\n\tv_fmac_f32 %5, %7, %1\n\tv_fmac_f32 %4, %8, %2\n\tv_fmac_f32 %5, %9, %3"
                         : "+v"(y[0]), "+v"(y[1]), "+v"(y[2]), "+v"(y[3]), "+v"(va[0]), "+v"(va[1])
                         : "v"(x[0]), "v"(x[1]), "v"(x[2]), "v"(x[3]), "v"(c.w2[4 * k]), "v"(c.w2[4 * k + 1]), "v"(c.w2[4 * k + 2]), "v"(c.w2[4 * k + 3]));
        }
        TE(c.tq[4])
        const float v = sum16(sum32(va[0] + va[1]));
        TS(c.ts[14])
        if (__builtin_amdgcn_inverse_ballot_w64(0xffffull)) deliver(c, v, 2 * nq, s2, s8, big, sums, outp);
        if (__builtin_expect(watch != 0ull, 0)) {
            const u32x4 e4 = LDS(const u32x4, OFF_NB + c.lane16), l4 = LDS(const u32x4, OFF_NB + 1024 + c.lane16);
            bool opn[4];
#pragma unroll
            for (int k = 0; k < 4; ++k) opn[k] = c.m1 == 0u && e4[k] == l4[k];
            bool own = nq == 0 && lane >= 9 && lane < 12 && LDSV(OFF_TAG + 512 + lane * 4) == c.tag;
            unsigned f = c.tag + 3u;
#pragma unroll 1
            for (int it = 0; it < (1 << 22) && __builtin_amdgcn_ballot_w64(opn[0] || opn[1] || opn[2] || opn[3] || own) != 0ull; ++it) {
                bool chg = false;
#pragma unroll
                for (int k = 0; k < 4; ++k)
                    if (opn[k] && __hip_atomic_load(ws32 + lane * 4 + k, __ATOMIC_RELAXED, __HIP_MEMORY_SCOPE_AGENT) != e4[k]) { opn[k] = false; chg = true; }
                if (own && __hip_atomic_load(ws32 + (c.tad >> 2), __ATOMIC_RELAXED, __HIP_MEMORY_SCOPE_AGENT) != c.tag) { own = false; chg = true; }
                if (__builtin_amdgcn_ballot_w64(chg) != 0ull) { publish(c, f); ++f; }
                else __builtin_amdgcn_s_sleep(2);
            }
        }
        FINISH(c)
        __builtin_amdgcn_endpgm(); __builtin_unreachable();
    } else {
        Cross& c = x;
#pragma unroll
        for (int q = 3; q < NP; ++q) { ia[q] = LDS(const u32x2, ib[(q - 1) & 3] + ((q - 1) >> 2) * 32); ic[q] = LDS(const u32x2, ib[(q - 1) & 3] + ((q - 1) >> 2) * 32 + 832); }
#pragma unroll
        for (int q = 3; q < NP; ++q) {
            c.ga[4 * q - 12] = ia[q][0] * 128u + srcA; c.ga[4 * q - 11] = ic[q][0] * 128u + srcB;
            c.ga[4 * q - 10] = ia[q][1] * 128u + srcA; c.ga[4 * q - 9] = ic[q][1] * 128u + srcB;
        }
#pragma unroll
        for (int k = 0; k < 40; ++k) PIN(c.ga[k]);
        {
            const unsigned ea = (unsigned)(cw * 4096 + l15 * 128);
            c.re[0] = ea + (unsigned)((g ^ (l15 >> 1)) * 16); c.re[1] = c.re[0] ^ 64u;
            c.rx = ea + (unsigned)(((2 * nq + (g >> 1)) ^ (l15 >> 1)) * 16);
            c.rx0[0] = c.rx + (unsigned)(((2 * g + rho) & 3) * 4); c.rx0[1] = c.rx + (unsigned)(((2 * g + rho + 1) & 3) * 4);
            c.six = (l15 & 1) ? 0xeeeeu : 0x4444u;
#pragma unroll
            for (int k = 0; k < 4; ++k) { const unsigned o = (l15 >> 1) == 2 * k + (g >> 1) ? 0x3f803f80u : 0u; c.on[k] = u32x4{o, o, o, o}; PIN(c.on[k]); }
            PIN(c.re[0]); PIN(c.re[1]); PIN(c.rx); PIN(c.rx0[0]); PIN(c.rx0[1]); PIN(c.six);
            zclear<3>(c); zclear<0>(c);
        }
        SB
        BARRIER;
        GATHER(2)
        TP(c.tq[2])
        {
            const int q = (lane >> 2) & 3, p = lane & 3;
            const unsigned a0 = (unsigned)(OFF_WBI + (8 * g + q) * 32 + p * 8), a1 = (unsigned)(OFF_WBI + (8 * g + q) * 32 + ((p + rho) & 3) * 8);
#pragma unroll
            for (int k = 0; k < 2; ++k) {
                c.wb[k] = CAT8(tr_read(a0 + k * 1024), tr_read(a0 + k * 1024 + 128));
                c.wr[k] = CAT8(tr_read(a1 + k * 1024), tr_read(a1 + k * 1024 + 128));
            }
#pragma unroll
            for (int k = 0; k < 2; ++k) { PIN(c.wb[k]); PIN(c.wr[k]); }
        }
        gp sums = (gp)c.ws + (unsigned)(WS_SUM + (s0 + 16 * cw) * 8), outp = out + (unsigned)((s0 + 16 * cw) * 2);
        PINS(sums); PINS(outp);
        TP(c.tq[3])
        SCOUT(1)
        WAITCNT(8, 0); BARRIER;
        TP(c.tq[4])
        __builtin_amdgcn_s_setprio(1);
        CStep<0>::run(c);
#define RDE(P) rdE<P, 0>(c); rdE<P, 1>(c); rdE<P, 2>(c); rdE<P, 3>(c);
        TS(c.ts[9])
#define CPIPE 0
#define CM6(P) cmm<P, 2>(c); cmm<P, 3>(c); cmm<P, 4>(c); cmm<P, 5>(c); cmm<P, 0>(c); cmm<P, 1>(c);
        RDE(9)
        if (Y1P == 4) mkY<1>(c);
        if (B0P == 1) bp0(c);
        if (CPIPE == 0) {
            CM6(9)
            if (Y1P == 3) mkY<1>(c);
            TX(1) WAITCNT(8, 15); SB TX(2)
            RDE(10)
            if (B0P == 2) bp0(c);
            cmm<10, 2>(c); cmm<10, 3>(c); cmm<10, 4>(c); cmm<10, 5>(c);
            rdX<10>(c);
            cmm<10, 0>(c); cmm<10, 1>(c);
            TX(3) WAITCNT(4, 0); SB
            RDE(11)
            swn<2, 0>(c);
            CM6(11)
            WAITCNT(0, 15); SB
            TS(c.ts[10])
            RDE(12)
            if (B0P == 3) bp0(c);
        } else {
            TX(1) WAITCNT(8, 15); SB TX(2)
            RDE(10)
            CM6(9)
            if (Y1P == 3) mkY<1>(c);
            if (B0P == 2) bp0(c);
            if (CPIPE == 1) { WAITCNT(4, 15); SB RDE(11) }
            cmm<10, 2>(c); cmm<10, 3>(c); cmm<10, 4>(c); cmm<10, 5>(c);
            rdX<10>(c);
            cmm<10, 0>(c); cmm<10, 1>(c);
            TX(3)
            if (CPIPE == 1) { WAITCNT(0, 0); SB TS(c.ts[10]) RDE(12) CM6(11) swn<2, 0>(c); }
            if (CPIPE == 2) { WAITCNT(4, 0); SB RDE(11) swn<2, 0>(c); WAITCNT(0, 15); SB TS(c.ts[10]) RDE(12) CM6(11) }
            if (CPIPE == 3) { WAITCNT(4, 0); SB RDE(11) swn<2, 0>(c); CM6(11) WAITCNT(0, 15); SB TS(c.ts[10]) RDE(12) }
            if (B0P == 3) bp0(c);
        }
        if (CBAR == 0) { BARRIER; TS(c.ts[11]) }
        u32x4 bq[4];
        u32x4 (&bp)[4] = c.bp;
        f32x2 T[6][8];
#define SPLIT(s0, s1, i0, i1) { _Pragma("unroll") for (int s = s0; s < s1; ++s) _Pragma("unroll") for (int i_ = i0; i_ < i1; ++i_) { \
            const f32x4 x = c.ew[2 * (1 + 4 * (s >> 1) + (i_ >> 1)) + (s & 1)]; T[s][i_] = f32x2{x[2 * (i_ & 1)], x[2 * (i_ & 1) + 1]}; } }
#define CB(B, i, h, k) { B[(2 * (i) + (h)) & 3][k] = cvt_o(T[2 * (k)][i][h], T[2 * (k) + 1][i][h]); }
#define SC(i, k) { asm volatile("v_pk_mul_f32 %0, %1, %0 op_sel_hi:[0,1]" : "+v"(T[2 * (k)][i]) : "v"(ao[k])); asm volatile("v_pk_mul_f32 %0, %1, %0 op_sel:[1,0]" : "+v"(T[2 * (k) + 1][i]) : "v"(ao[k])); }
        SPLIT(0, 4, 0, 8)
        SB
        cmm<12, 0>(c); cmm<12, 1>(c);
        STAMP(2)
        TS(c.ts[12])
        cmm<12, 2>(c); cmm<12, 3>(c);
        rdX<12>(c);
        if (CBAR == 1) { BARRIER; TS(c.ts[11]) }
        zpack<2>(c);
        cmm<12, 4>(c); cmm<12, 5>(c);
        const f32x4 hp = mf0(c.s1, c.zq);
        if (CBAR == 2) { BARRIER; TS(c.ts[11]) }
        asm volatile("s_nop 6");
        swn<2, 1, 0, 2>(c);
        float hq[4];
        asm volatile("v_mul_f32 %0, %4, %4\n\tv_mul_f32 %1, %5, %5\n\tv_mul_f32 %2, %6, %6\n\tv_mul_f32 %3, %7, %7\n\t"
                     "v_fma_f32 %0, %0, %9, %8\n\tv_fma_f32 %1, %1, %9, %8\n\tv_fma_f32 %2, %2, %9, %8\n\tv_fma_f32 %3, %3, %9, %8\n\t"
                     "v_mul_f32 %0, %0, %4\n\tv_mul_f32 %1, %1, %5\n\tv_mul_f32 %2, %2, %6\n\tv_mul_f32 %3, %3, %7\n\t"
                     "v_exp_f32 %0, %0\n\tv_exp_f32 %1, %1\n\tv_exp_f32 %2, %2\n\tv_exp_f32 %3, %3\n\t"
                     "v_fma_f32 %0, %0, %10, %10\n\tv_fma_f32 %1, %1, %10, %10\n\tv_fma_f32 %2, %2, %10, %10\n\tv_fma_f32 %3, %3, %10, %10\n\t"
                     "v_rcp_f32 %0, %0\n\tv_rcp_f32 %1, %1\n\tv_rcp_f32 %2, %2\n\tv_rcp_f32 %3, %3\n\t"
                     "v_mul_f32 %0, %0, %4\n\tv_mul_f32 %1, %1, %5\n\tv_mul_f32 %2, %2, %6\n\tv_mul_f32 %3, %3, %7\n\t"
                     "v_cvt_pk_bf16_f32 %0, %0, %1\n\tv_cvt_pk_bf16_f32 %1, %2, %3"
                     : "=&v"(hq[0]), "=&v"(hq[1]), "=&v"(hq[2]), "=&v"(hq[3]) : "v"(hp[0]), "v"(hp[1]), "v"(hp[2]), "v"(hp[3]), "v"(c.k0), "v"(c.k1), "v"(c.k2));
        if (SPC == 2) SPOT(1)
        if (CBAR == 3) { BARRIER; TS(c.ts[11]) }
        const f32x2 hb = {hq[0], hq[1]};
        swn<2, 1, 2, 4>(c);
        f32x4 av[2];
        asm volatile("v_mfma_f32_16x16x16_bf16 %0, %1, %2, 0" : "=&v"(av[0]) : "v"(c.wa[0]), "v"(hb));
        asm volatile("v_mfma_f32_16x16x16_bf16 %0, %1, %2, 0" : "=&v"(av[1]) : "v"(c.wa[1]), "v"(hb));
        if (CBAR == 4) { BARRIER; TS(c.ts[11]) }
        swn<2, 1, 4, 8>(c);
        if (CBAR == 5) { BARRIER; TS(c.ts[11]) }
        swn<2, 2, 0, 4>(c);
        if (CBAR == 6) { BARRIER; TS(c.ts[11]) }
        TS(c.ts[13])
        SB
        float as[8];
#pragma unroll
        for (int r = 0; r < 8; ++r) as[r] = exp_o(av[r >> 2][r & 3]);
        asm volatile("v_add_f32 %0, 1.0, %0\n\tv_add_f32 %1, 1.0, %1\n\tv_add_f32 %2, 1.0, %2\n\tv_add_f32 %3, 1.0, %3\n\tv_add_f32 %4, 1.0, %4\n\tv_add_f32 %5, 1.0, %5\n\tv_add_f32 %6, 1.0, %6\n\tv_add_f32 %7, 1.0, %7\n\t"
                     "v_rcp_f32 %0, %0\n\tv_rcp_f32 %1, %1\n\tv_rcp_f32 %2, %2\n\tv_rcp_f32 %3, %3\n\tv_rcp_f32 %4, %4\n\tv_rcp_f32 %5, %5\n\tv_rcp_f32 %6, %6\n\tv_rcp_f32 %7, %7"
                     : "+v"(as[0]), "+v"(as[1]), "+v"(as[2]), "+v"(as[3]), "+v"(as[4]), "+v"(as[5]), "+v"(as[6]), "+v"(as[7]));
        unsigned flg;
        SB
        if (CBAR == 7) { BARRIER; TS(c.ts[11]) }
        swn<2, 2, 4, 8>(c);
        SPLIT(4, 6, 0, 2) SPLIT(4, 6, 4, 6)
#define WSEQ(n) LDS(const u32x4, c.wf[(((n) >> 4) & 1) ^ 1] + ((n) >> 5) * SLOT + ((n) & 15) * 1024)
        u32x4 wq[64];
#define FIRST { TE(c.tq[0]) SB WAITCNT(63, 0); BARRIER; TX(6) SB _Pragma("unroll") for (int r = 0; r < WPRE; ++r) wq[r] = WSEQ(r); SB }
        if (!PREP) FIRST
        CB(bp, 0, 0, 2) CB(bp, 0, 1, 2)
#define MU(x, a) { x = f32x2{mul_o(x[0], a), mul_o(x[1], a)}; }
        f32x2 ao[4] = {{as[0], as[1]}, {as[2], as[3]}, {as[4], as[5]}, {as[6], as[7]}};
        {
            f32x2 m[4] = {{c.ew[0][0], c.ew[0][1]}, {c.ew[1][0], c.ew[1][1]}, {c.ew[0][2], c.ew[0][3]}, {c.ew[1][2], c.ew[1][3]}};
#pragma unroll
            for (int r = 0; r < 4; r += 2) {
                if (NOPK) { MU(m[r], as[6]) MU(m[r + 1], as[7]) }
                else {
                    asm volatile("v_pk_mul_f32 %0, %1, %0 op_sel_hi:[0,1]" : "+v"(m[r]) : "v"(ao[3]));
                    asm volatile("v_pk_mul_f32 %0, %1, %0 op_sel:[1,0]" : "+v"(m[r + 1]) : "v"(ao[3]));
                }
            }
            if (NOPK) { _Pragma("unroll") for (int r = 0; r < 6; ++r) MU(T[r][0], as[r]) }
            else { SC(0, 0) SC(0, 1) SC(0, 2) }
#pragma unroll
            for (int r = 0; r < 4; ++r) bq[r][3] = cvt_o(m[r & 2][r & 1], m[(r & 2) + 1][r & 1]);
        }
        SB
        if (SPC == 1) SPOT(1)
        if (Y2P == 0) { WAITCNT(63, 0); SB mkY<2>(c); }
        if (SPC == 0) SPOT(1)
        if (PREP) { FIRST swn<2, 3>(c); SB }
        if (Y2P == 1) mkY<2>(c);
        TE(c.tq[1])
        f32x4 dg;
        unsigned hix;
        f32x4 u[2][4];
        f32x2 sv[3][4], l0, l1, l2, l3;
#define UP(h, k) f32x2{u[h][(k) >> 1][2 * ((k) & 1)], u[h][(k) >> 1][2 * ((k) & 1) + 1]}
#define FM(i, h, k) asm volatile("v_pk_fma_f32 %0, %1, %2, %3" : "=v"(sv[k][2 * ((i) & 1) + (h)]) : "v"(UP(h, 4 + (k))), "v"(ao[k]), "v"(UP(h, k)));
#define FM3(i, a, b) asm volatile("s_mov_b64 exec, %7\n\tv_pk_fma_f32 %0, %2, %6, %3\n\tv_pk_fma_f32 %1, %4, %6, %5\n\ts_mov_b64 exec, -1" : "+v"(a), "+v"(b) \
                            : "v"(UP(0, 7)), "v"(UP(0, 3)), "v"(UP(1, 7)), "v"(UP(1, 3)), "v"(ao[3]), "s"(0xffffull << ((((i) >> 1) - rho) & 3) * 16));
#define DOT4(y, a, b, c_, d) asm volatile("v_smfmac_f32_16x16x64_bf16 %0, %1, %2, %3" : "+v"(dg) : "v"(c.Y[y]), "v"(f32x8{a[0], a[1], b[0], b[1], c_[0], c_[1], d[0], d[1]}), "v"(hix));
#define DOT(q, k) DOT4(4 * (k) + (q), sv[k][0], sv[k][1], sv[k][2], sv[k][3])
#pragma unroll
        for (int i = 0; i < 8; ++i) {
            static_assert(dops(15) == 4, "");
            constexpr int f3 = OFF_FL - OFF_B - SLOT, f5 = OFF_FL + 2048 - OFF_B - SLOT;
            if (i == 2) { TE(c.tq[2]) }
            if (i == 4) { TE(c.tq[3]) }
            if (i == 1) { SPLIT(4, 6, 2, 4) SPLIT(4, 6, 6, 8) }
#pragma unroll
            for (int j = 0; j < 8; ++j) {
                const int n = 8 * i + j, h = j >> 2;
                const bool nx = i < 7;
                SB
                if (!(j & 1)) { if (n + WPRE <= 64) WAITCNT(63, WPRE - 2); SB }
                u[h][j & 3] = mf0(wq[n], (j & 2) ? bq[(2 * i + h) & 3] : bp[(2 * i + h) & 3]);
                SB
                if (n == 5) flg = LDSV(c.wf[1] + f3);
                if (n + WPRE == 16) { SB TX(5) wait_set(flg, c.wf[1] + f3); SB TX(4) }
                if (n == 37 && f3 != f5) flg = LDSV(c.wf[1] + f5);
                if (n + WPRE == 48 && f3 != f5) { SB wait_set(flg, c.wf[1] + f5); SB }
                if (n + WPRE < 64) wq[n + WPRE] = WSEQ(n + WPRE);
                SB
                if (i == 0 && !PREP) { if (j == 2) swn<2, 3, 0, 1>(c); if (j == 3) swn<2, 3, 1, 2>(c); if (j == 4) swn<2, 3, 2, 4>(c); if (j == 5) swn<2, 3, 4, 5>(c); if (j == 6) swn<2, 3, 5, 7>(c); if (j == 7) swn<2, 3, 7, 8>(c); }
                const bool dt = i && !(i & 1);
                if (j == 0) { CB(bq, i, 0, 0) CB(bq, i, 0, 1) }
                if (j == 1) { CB(bq, i, 0, 2) CB(bq, i, 1, 0) CB(bq, i, 1, 1) }
                if (j == 2) { CB(bq, i, 1, 2) if (dt) DOT(i / 2 - 1, 0) }
                if (j == 3) { if (nx && i) CB(bp, i + 1, 0, 0) if (dt) DOT(i / 2 - 1, 1) }
                if (j == 4) { if (nx && i) CB(bp, i + 1, 1, 0) if (dt) DOT(i / 2 - 1, 2) }
                if (j == 5) { if (nx && i) CB(bp, i + 1, 0, 1) if (Y2P == 2 && i < 2) { if (i) mkY<2, 2, 3>(c); else mkY<2, 0, 1>(c); } }
                if (j == 6) { if (nx && i) CB(bp, i + 1, 1, 1) if (Y2P == 2 && i < 2) { if (i) mkY<2, 3, 4>(c); else mkY<2, 1, 2>(c); } }
                if (j == 7) {
                    if (nx) { CB(bp, i + 1, 0, 2) CB(bp, i + 1, 1, 2) }
                    FM(i, 0, 0) FM(i, 0, 1) FM(i, 0, 2)
                    if (nx) { SC(i + 1, 0) SC(i + 1, 1) SC(i + 1, 2) } else asm volatile("s_nop 4");
                    if (i == 0) asm volatile("" : "=v"(l0), "=v"(l1));
                    if (i == 1) asm volatile("" : "=v"(l2), "=v"(l3));
                    FM(i, 1, 0) FM(i, 1, 1) FM(i, 1, 2)
                    if (i & 1) { FM3(i, l2, l3) } else { FM3(i, l0, l1) }
                    if (i == 1) {
                        f32x2 z0, z1;
                        asm volatile("v_mov_b64 %0, 0\n\tv_mov_b64 %1, 0\n\tv_mov_b32 %2, 0xdddd" : "=v"(z0), "=v"(z1), "=v"(hix));
                        dg = f32x4{z0[0], z0[1], z1[0], z1[1]};
                    }
                }
            }
        }
        DOT(3, 0)
        unsigned s2;
        asm volatile("v_mbcnt_lo_u32_b32 %0, -1, 0\n\tv_mbcnt_hi_u32_b32 %0, -1, %0" : "=v"(s2));
        DOT(3, 1)
        asm volatile("v_lshlrev_b32 %0, 1, %0\n\tv_and_b32 %0, 30, %0" : "+v"(s2));
        DOT(3, 2)
        unsigned s8 = s2 * 4u;
        double big = 0x1.8p52;
        PIN(s8); PIN(big);
        SB
        DOT4(12, l0, l1, l2, l3)
        float v, v2;
        asm volatile("s_mov_b64 vcc, %6\n\ts_nop 6\n\tv_cndmask_b32 %0, %2, %3, vcc\n\tv_cndmask_b32 %1, %4, %5, vcc\n\tv_cndmask_b32 %0, %0, %1, %7"
                     : "=&v"(v), "=&v"(v2) : "v"(dg[0]), "v"(dg[1]), "v"(dg[2]), "v"(dg[3]), "s"(0xaaaaaaaaaaaaaaaaull), "s"(0xccccccccccccccccull) : "vcc");
        TE(c.tq[4])
        STAMP(4)
        TS(c.ts[14])
        if (__builtin_amdgcn_inverse_ballot_w64(0xf0000f0000f0000full)) deliver(c, v * (1.0f + 1.0f / 384.0f), 2 * nq + 1, s2, s8, big, sums, outp);
        const int ln = __builtin_amdgcn_mbcnt_hi(~0u, __builtin_amdgcn_mbcnt_lo(~0u, 0u));
        const int lane = ln, tid = w * 64 + ln;
        (void)lane; (void)tid;
        FINISH(c)
        asm volatile("s_endpgm\n" PADTO(2));
        __builtin_amdgcn_endpgm(); __builtin_unreachable();
    }
}

extern "C" void kernel_launch(void* const* d_in, const int* in_sizes, int n_in,
                              void* d_out, int out_size, void* d_ws, size_t ws_size,
                              hipStream_t stream) {
    const int* idx = (const int*)d_in[0];
    const uint16_t* table = (const uint16_t*)d_in[1];
    const uint16_t* se_w1 = (const uint16_t*)d_in[2];
    const uint16_t* se_w2 = (const uint16_t*)d_in[3];
    const uint16_t* w_bi = (const uint16_t*)d_in[4];
    const uint16_t* w_cross = (const uint16_t*)d_in[5];
    const uint16_t* w_deep1 = (const uint16_t*)d_in[6];
    const uint16_t* w_deep2 = (const uint16_t*)d_in[7];
    (void)hipFuncSetAttribute((const void*)k_main, hipFuncAttributeMaxDynamicSharedMemorySize, LDS_T);
    k_main<<<256, 512, LDS_T, stream>>>(idx, table, w_bi, w_deep1, w_cross, (unsigned*)d_ws, se_w1, se_w2, w_deep2, (__hip_bfloat16*)d_out, (unsigned long long*)((char*)d_ws + (128ull << 20)));
}
```
